```python
import torch

def _softmax(x, axis=-1):
    return torch.softmax(x, dim=axis)
def _gelu(x):
    return torch.nn.functional.gelu(x, approximate="tanh")
def _silu(x):
    return torch.nn.functional.silu(x)
def _sigmoid(x):
    return torch.sigmoid(x)
def _layernorm(x, axis=-1, eps=1e-5):
    m = x.mean(dim=axis, keepdim=True)
    v = x.var(dim=axis, keepdim=True, unbiased=False)
    return (x - m) / torch.sqrt(v + eps)
def _scatter_add(tgt, idx, src, axis=0):
    return tgt.clone().index_add_(axis, idx, src)
def _segment_sum(src, seg, n):
    out = torch.zeros((n,) + src.shape[1:], dtype=src.dtype)
    return out.index_add_(0, seg, src)
def _pad(x, pw):
    flat = tuple(v for pair in reversed(pw) for v in pair)
    return torch.nn.functional.pad(x, flat)

kernel_name = 'concat_heads__ssm_conv_s4d__linear_kernel_hedgehog__prompt'

def setup_inputs():

    _w = (lambda a: (lambda t: t.to(torch.bfloat16) if t.is_floating_point() else t)(torch.as_tensor(a)))
    def _A_setup():
        Bsz, L, D, N = 1, 2048, 448, 256
        return {
            "u": _w((lambda *s: torch.randn(*s, dtype=torch.bfloat16) * 0.05)(Bsz, L, D)),
            "log_A_real": _w((lambda *s: torch.randn(*s, dtype=torch.bfloat16) * 6.4)(D, N) * 0.5),
            "A_imag": _w((lambda *s: torch.randn(*s, dtype=torch.bfloat16) * 0.2)(D, N) * 3.14),
            "C_real": _w((lambda *s: torch.randn(*s, dtype=torch.bfloat16) * 0.8)(D, N) / N ** 0.5),
            "C_imag": _w((lambda *s: torch.randn(*s, dtype=torch.bfloat16) * 0.8)(D, N) / N ** 0.5),
            "log_dt": _w((lambda *s: torch.randn(*s, dtype=torch.bfloat16) * 0.2)(D) * 0.5 - 4.0),
            "D_skip": _w((lambda *s: torch.randn(*s, dtype=torch.bfloat16) * 0.2)(D)),
        }
    _A_in = _A_setup()

    def _B_setup():
        B, S, D, H = 1, 2048, 448, 16
        Dh = D // H
        return {
            "x": _w((lambda *s: torch.randn(*s, dtype=torch.bfloat16) * 0.4)(B, S, D)),
            "wqkv": _w((lambda *s: torch.randn(*s, dtype=torch.bfloat16) * 0.4)(D, 3 * D)),
            "w_phi": _w((lambda *s: torch.randn(*s, dtype=torch.bfloat16) * 0.4)(H, Dh, Dh) * (Dh ** -0.5)),
            "wo": _w((lambda *s: torch.randn(*s, dtype=torch.bfloat16) * 0.05)(D, D)),
        }
    _B_in = _B_setup()

    return {
        "A_u": _A_in["u"],
        "A_log_A_real": _A_in["log_A_real"],
        "A_A_imag": _A_in["A_imag"],
        "A_C_real": _A_in["C_real"],
        "A_C_imag": _A_in["C_imag"],
        "A_log_dt": _A_in["log_dt"],
        "A_D_skip": _A_in["D_skip"],
        "B_x": _B_in["x"],
        "B_wqkv": _B_in["wqkv"],
        "B_w_phi": _B_in["w_phi"],
        "B_wo": _B_in["wo"],
    }

def reference(A_u, A_log_A_real, A_A_imag, A_C_real, A_C_imag, A_log_dt, A_D_skip, B_x, B_wqkv, B_w_phi, B_wo):
    def _A_ref(u, log_A_real, A_imag, C_real, C_imag, log_dt, D_skip):
        L = u.shape[1]
        dt = torch.exp(log_dt)
        A = -torch.exp(log_A_real) + 1j * A_imag
        C = (C_real + 1j * C_imag) * dt[:, None]
        pos = (lambda x, dt: x.to(dt))(torch.arange(L), log_dt.dtype)
        P = torch.exp((A * dt[:, None])[:, :, None] * pos[None, None, :])
        K = 2.0 * torch.einsum("dn,dnl->dl", C, P).real
        L2 = 2 * L
        u_t = (lambda x, *d: x.permute(*d))(u, 0, 2, 1)
        Kf = (lambda x, *a, **k: torch.fft.rfft(x.float(), *a, **k))(K, L2)
        Uf = (lambda x, *a, **k: torch.fft.rfft(x.float(), *a, **k))(u_t, L2)
        y = (lambda x, *a, **k: torch.fft.irfft(x, *a, **k).to(torch.bfloat16))(Uf * Kf[None, :, :], L2)[:, :, :L]
        y = (lambda x, *d: x.permute(*d))(y, 0, 2, 1)
        return y + u * D_skip[None, None, :]

    def _B_ref(x, wqkv, w_phi, wo):
        B, S, D = x.shape
        H = 16; Dh = D // H
        qkv = (lambda x, *d: x.permute(*d))((x @ wqkv).reshape(B, S, 3, H, Dh), 2, 0, 3, 1, 4)
        q, k, v = qkv[0], qkv[1], qkv[2]
        pq = torch.einsum("bhsd,hdf->bhsf", q, w_phi)
        pk = torch.einsum("bhsd,hdf->bhsf", k, w_phi)
        phi_q = torch.concatenate([_softmax(pq, axis=-1), _softmax(-pq, axis=-1)], axis=-1)
        phi_k = torch.concatenate([_softmax(pk, axis=-1), _softmax(-pk, axis=-1)], axis=-1)
        kv = torch.einsum("bhsf,bhsd->bhfd", phi_k, v)
        z  = torch.einsum("bhsf->bhf", phi_k)
        num = torch.einsum("bhqf,bhfd->bhqd", phi_q, kv)
        den = torch.einsum("bhqf,bhf->bhq", phi_q, z)[..., None] + 1e-6
        o = num / den
        return (lambda x, *d: x.permute(*d))(o, 0, 2, 1, 3).reshape(B, S, D) @ wo

    a = _A_ref(A_u, A_log_A_real, A_A_imag, A_C_real, A_C_imag, A_log_dt, A_D_skip)
    b = _B_ref(B_x, B_wqkv, B_w_phi, B_wo)
    return torch.concatenate([a, b], axis=-1)
```

```cpp
#include <hip/hip_runtime.h>
#include <hip/hip_bf16.h>
#include <stdint.h>

using bf16x8 = __attribute__((ext_vector_type(8))) short;
using s16x4  = __attribute__((ext_vector_type(4))) short;
using f32x16 = __attribute__((ext_vector_type(16))) float;
using f32x4  = __attribute__((ext_vector_type(4))) float;
using u32x4  = __attribute__((ext_vector_type(4))) unsigned;
using u32x2  = __attribute__((ext_vector_type(2))) unsigned;
typedef __bf16 bfx2_t __attribute__((ext_vector_type(2)));
typedef float  f32x2  __attribute__((ext_vector_type(2)));

#define DI __device__ __forceinline__
#define LAS __attribute__((address_space(3)))
#define MFMA32(a, b, c) __builtin_amdgcn_mfma_f32_32x32x16_bf16((a), (b), (c), 0, 0, 0)
#define MFMA16(a, b, c) __builtin_amdgcn_mfma_f32_16x16x32_bf16((a), (b), (c), 0, 0, 0)

constexpr int L_  = 2048;
constexpr int D_  = 448;
constexpr int N_  = 256;
constexpr int H_  = 16;
constexpr int OUTW = 896;

DI unsigned pack_bf16(float lo, float hi) {
  f32x2 v = {lo, hi};
  return __builtin_bit_cast(unsigned, __builtin_convertvector(v, bfx2_t));
}
DI float bf_lo(unsigned p) { return __uint_as_float(p << 16); }
DI float bf_hi(unsigned p) { return __uint_as_float(p & 0xffff0000u); }
DI float bf2f(unsigned short b) { return __uint_as_float(((unsigned)b) << 16); }
DI float bfr(float x) { return bf_lo(pack_bf16(x, 0.f)); }
DI s16x4 tr_read(const char* p) {
  return __builtin_amdgcn_ds_read_tr16_b64_v4i16((LAS s16x4*)(p));
}
DI bf16x8 tr_frag(const char* p0, const char* p1) {
  s16x4 a = tr_read(p0), b = tr_read(p1);
  return __builtin_shufflevector(a, b, 0, 1, 2, 3, 4, 5, 6, 7);
}
DI bf16x8 as_frag(u32x4 v) { return __builtin_bit_cast(bf16x8, v); }
DI void glds16(const void* g, void* lds_wave_base) {
  __builtin_amdgcn_global_load_lds((const unsigned*)g, (unsigned*)lds_wave_base, 16, 0, 0);
}
DI void wait_vm0() { asm volatile("s_waitcnt vmcnt(0)" ::: "memory"); }

constexpr int A_UCOPY = 4160;
constexpr int A_US    = 0;
constexpr int A_KRH   = A_US + 4 * A_UCOPY;
constexpr int A_KRL   = A_KRH + 4576;
constexpr int A_KD    = A_KRL + 4576;
constexpr int A_PRM   = A_KD + 4112;
constexpr int A_RED   = A_PRM + 4096;
constexpr int A_SUM   = A_RED + 32768;
constexpr int A_LDS   = A_SUM + 64;

DI void cexp_eval(float lr, float li, float t, float& er, float& ei) {
  const float xr = lr * t;
  const float e  = __builtin_amdgcn_exp2f(xr * 1.44269504088896340736f);
  const float x  = li * t;
  const float C_HI = 0.15915494309189533577f;
  const float C_LO = (float)(0.15915494309189533577 - (double)0.15915494309189533577f);
  const float rh = x * C_HI;
  float rl = __builtin_fmaf(x, C_HI, -rh);
  rl = __builtin_fmaf(x, C_LO, rl);
  const float rev = (rh - __builtin_rintf(rh)) + rl;
  er = e * __builtin_amdgcn_cosf(rev);
  ei = e * __builtin_amdgcn_sinf(rev);
}
DI void split2(float a, float b, unsigned& hi, unsigned& lo) {
  hi = pack_bf16(a, b);
  lo = pack_bf16(a - bf_lo(hi), b - bf_hi(hi));
}
DI int kd_index(int tap) {
  unsigned u = __float_as_uint((float)tap);
  u = (u + 0x7fffu + ((u >> 16) & 1u)) & 0xffff0000u;
  const int p = (int)__uint_as_float(u);
  if (p < 256) return p;
  if (p >= 2048) return 1024;
  const int s = (31 - __builtin_clz(p)) - 7;
  return s * 256 + (p >> s);
}

DI void part_a_block(char* sm, int d0, const unsigned short* __restrict__ u,
                     const unsigned short* __restrict__ logA, const unsigned short* __restrict__ Aim,
                     const unsigned short* __restrict__ Cre, const unsigned short* __restrict__ Cim,
                     const unsigned short* __restrict__ logdt, const unsigned short* __restrict__ Dsk,
                     unsigned short* __restrict__ out) {
  const int tid = threadIdx.x, lane = tid & 63, w = tid >> 6;
  const int i16 = lane & 15, g = lane >> 4;
  {
    unsigned uv[8];
#pragma unroll
    for (int i = 0; i < 8; ++i) uv[i] = *(const unsigned*)(u + (size_t)(tid + 256 * i) * D_ + d0);
    if (tid < 64) {
      const int c = tid >> 4, e = tid & 15;
      ((unsigned short*)(sm + A_US + c * A_UCOPY))[e] = 0;
      ((unsigned short*)(sm + A_US + c * A_UCOPY))[2064 + e] = 0;
    }
    __syncthreads();
#pragma unroll
    for (int i = 0; i < 8; ++i) {
      const int l = tid + 256 * i;
      const unsigned short v0 = (unsigned short)(uv[i] & 0xffffu), v1 = (unsigned short)(uv[i] >> 16);
      ((unsigned short*)(sm + A_US + 0 * A_UCOPY))[l + 16] = v0;
      ((unsigned short*)(sm + A_US + 1 * A_UCOPY))[l + 15] = v0;
      ((unsigned short*)(sm + A_US + 2 * A_UCOPY))[l + 16] = v1;
      ((unsigned short*)(sm + A_US + 3 * A_UCOPY))[l + 15] = v1;
    }
  }
  f32x4 yres[2][2];
#pragma unroll 1
  for (int ch = 0; ch < 2; ++ch) {
    const int d = d0 + ch;
    {
      const int n = tid;
      const float dt = bfr(expf(bf2f(logdt[d])));
      const float a  = bfr(expf(bf2f(logA[d * N_ + n])));
      const float lr = -a * dt, li = bf2f(Aim[d * N_ + n]) * dt;
      const float cr = bf2f(Cre[d * N_ + n]) * dt, ci = bf2f(Cim[d * N_ + n]) * dt;
      *(f32x4*)(sm + A_PRM + n * 16) = f32x4{lr, li, cr, ci};
      float er, ei;
      cexp_eval(lr, li, 2048.f, er, ei);
      float t = cr * er - ci * ei;
#pragma unroll
      for (int o = 32; o >= 1; o >>= 1) t += __shfl_xor(t, o);
      if (lane == 0) ((float*)(sm + A_SUM))[w] = t;
    }
    __syncthreads();
    {
      f32x4 acc[4];
#pragma unroll
      for (int s = 0; s < 4; ++s) acc[s] = f32x4{0.f, 0.f, 0.f, 0.f};
      const float ty = (float)i16, tx = (float)(16 * i16);
#pragma unroll 1
      for (int ks = 0; ks < 4; ++ks) {
        float yr[4], yi[4], xr[4], xi[4], cr[4], ci[4];
#pragma unroll
        for (int m = 0; m < 4; ++m) {
          const f32x4 p = *(const f32x4*)(sm + A_PRM + (64 * w + 16 * ks + 4 * g + m) * 16);
          cexp_eval(p[0], p[1], ty, yr[m], yi[m]);
          cexp_eval(p[0], p[1], tx, xr[m], xi[m]);
          cr[m] = p[2]; ci[m] = p[3];
        }
#pragma unroll
        for (int s = 0; s < 4; ++s) {
          u32x4 ah, al, bh, bl;
#pragma unroll
          for (int m = 0; m < 4; ++m) {
            unsigned h, l;
            split2(yr[m], yi[m], h, l); ah[m] = h; al[m] = l;
            const float zr = cr[m] * xr[m] - ci[m] * xi[m];
            const float zi = cr[m] * xi[m] + ci[m] * xr[m];
            split2(zr, -zi, h, l); bh[m] = h; bl[m] = l;
          }
          acc[s] = MFMA16(as_frag(ah), as_frag(bh), acc[s]);
          acc[s] = MFMA16(as_frag(ah), as_frag(bl), acc[s]);
          acc[s] = MFMA16(as_frag(al), as_frag(bh), acc[s]);
          if (s < 3) {
#pragma unroll
            for (int m = 0; m < 4; ++m) {
              const float a = yr[m], b = yi[m];
              yr[m] = a * a - b * b; yi[m] = 2.f * a * b;
              const float c = xr[m], e = xi[m];
              xr[m] = c * c - e * e; xi[m] = 2.f * c * e;
            }
          }
        }
      }
#pragma unroll
      for (int s = 0; s < 4; ++s)
        *(f32x4*)(sm + A_RED + ((w * 4 + s) * 256 + 16 * i16 + 4 * g) * 4) = acc[s];
    }
    __syncthreads();
    {
      const float* red = (const float*)(sm + A_RED);
      float* kd = (float*)(sm + A_KD);
#pragma unroll
      for (int s = 0; s < 4; ++s)
        kd[s * 256 + tid] = 2.f * ((red[(0 * 4 + s) * 256 + tid] + red[(1 * 4 + s) * 256 + tid]) +
                                   (red[(2 * 4 + s) * 256 + tid] + red[(3 * 4 + s) * 256 + tid]));
      if (tid == 0) {
        const float* sp = (const float*)(sm + A_SUM);
        kd[1024] = 2.f * ((sp[0] + sp[1]) + (sp[2] + sp[3]));
      }
    }
    __syncthreads();
    {
      const float* kd = (const float*)(sm + A_KD);
      for (int c = tid; c < 286; c += 256) {
        float v[8];
#pragma unroll
        for (int j = 0; j < 8; ++j) {
          const int tap = 2047 - (8 * c + j);
          v[j] = (tap >= 0) ? kd[kd_index(tap < 0 ? 0 : tap)] : 0.f;
        }
        u32x4 h, l;
#pragma unroll
        for (int j = 0; j < 4; ++j) { unsigned a, b; split2(v[2 * j], v[2 * j + 1], a, b); h[j] = a; l[j] = b; }
        *(u32x4*)(sm + A_KRH + c * 16) = h;
        *(u32x4*)(sm + A_KRL + c * 16) = l;
      }
    }
    __syncthreads();
    {
      f32x4 acc[8];
#pragma unroll
      for (int q = 0; q < 8; ++q) acc[q] = f32x4{0.f, 0.f, 0.f, 0.f};
      const int par = (i16 + 1) & 1;
      const char* ubase = sm + A_US + (2 * ch + par) * A_UCOPY + 2 * (i16 + 1 - par + 8 * g);
#pragma unroll 1
      for (int cc = 0; cc < 2; ++cc) {
        const int r = 2 * w + cc;
        bf16x8 af[8];
#pragma unroll
        for (int a = 0; a < 8; ++a) {
          const unsigned* p = (const unsigned*)(ubase + 64 * (r + 8 * a));
          u32x4 v; v[0] = p[0]; v[1] = p[1]; v[2] = p[2]; v[3] = p[3];
          af[a] = as_frag(v);
        }
        const int kb = 2 * (2032 - 16 * i16 + 32 * r + 8 * g);
#pragma unroll
        for (int b = 0; b < 8; ++b) {
          const bf16x8 bh = *(const bf16x8*)(sm + A_KRH + kb - 512 * b);
          const bf16x8 bl = *(const bf16x8*)(sm + A_KRL + kb - 512 * b);
#pragma unroll
          for (int a = 0; a + b < 8; ++a) {
            acc[a + b] = MFMA16(af[a], bh, acc[a + b]);
            acc[a + b] = MFMA16(af[a], bl, acc[a + b]);
          }
        }
      }
#pragma unroll
      for (int q = 0; q < 8; ++q) *(f32x4*)(sm + A_RED + ((w * 8 + q) * 64 + lane) * 16) = acc[q];
    }
    __syncthreads();
#pragma unroll
    for (int qq = 0; qq < 2; ++qq) {
      const int q = 2 * w + qq;
      f32x4 s0 = *(const f32x4*)(sm + A_RED + ((0 * 8 + q) * 64 + lane) * 16);
      f32x4 s1 = *(const f32x4*)(sm + A_RED + ((1 * 8 + q) * 64 + lane) * 16);
      f32x4 s2 = *(const f32x4*)(sm + A_RED + ((2 * 8 + q) * 64 + lane) * 16);
      f32x4 s3 = *(const f32x4*)(sm + A_RED + ((3 * 8 + q) * 64 + lane) * 16);
      f32x4 y = (s0 + s1) + (s2 + s3);
      if (ch == 0) yres[0][qq] = y; else yres[1][qq] = y;
    }
    __syncthreads();
  }
  const float ds0 = bf2f(Dsk[d0]), ds1 = bf2f(Dsk[d0 + 1]);
#pragma unroll
  for (int qq = 0; qq < 2; ++qq) {
#pragma unroll
    for (int e = 0; e < 4; ++e) {
      const int l = 256 * (2 * w + qq) + 16 * i16 + 4 * g + e;
      const float u0 = bf2f(((const unsigned short*)(sm + A_US + 0 * A_UCOPY))[l + 16]);
      const float u1 = bf2f(((const unsigned short*)(sm + A_US + 2 * A_UCOPY))[l + 16]);
      const float o0 = bfr(yres[0][qq][e]) + bfr(u0 * ds0);
      const float o1 = bfr(yres[1][qq][e]) + bfr(u1 * ds1);
      *(unsigned*)(out + (size_t)l * OUTW + d0) = pack_bf16(o0, o1);
    }
  }
}

DI int crow(int reg, int hh) { return (reg & 3) + 8 * (reg >> 2) + 4 * hh; }

DI bf16x8 pack_acc(const f32x16& x, int s) {
  u32x4 p;
#pragma unroll
  for (int j = 0; j < 4; ++j) p[j] = pack_bf16(x[8 * s + 2 * j], x[8 * s + 2 * j + 1]);
  return as_frag(p);
}

DI void dual_softmax(const f32x16& pacc, int hh, u32x4 (&pos)[2], u32x4 (&neg)[2]) {
  float p[16];
#pragma unroll
  for (int r = 0; r < 16; ++r) p[r] = bfr(pacc[r]);
  float mx = p[0], mn = p[0];
#pragma unroll
  for (int r = 1; r < 12; ++r) { mx = fmaxf(mx, p[r]); mn = fminf(mn, p[r]); }
  if (hh == 0) {
#pragma unroll
    for (int r = 12; r < 16; ++r) { mx = fmaxf(mx, p[r]); mn = fminf(mn, p[r]); }
  }
  mx = fmaxf(mx, __shfl_xor(mx, 32));
  mn = fminf(mn, __shfl_xor(mn, 32));
  float ep[16], en[16], sp = 0.f, sn = 0.f;
#pragma unroll
  for (int r = 0; r < 16; ++r) {
    const bool valid = (r < 12) || (hh == 0);
    ep[r] = valid ? __expf(p[r] - mx) : 0.f;
    en[r] = valid ? __expf(mn - p[r]) : 0.f;
    sp += ep[r]; sn += en[r];
  }
  sp += __shfl_xor(sp, 32);
  sn += __shfl_xor(sn, 32);
  const float rp = 1.f / sp, rn = 1.f / sn;
#pragma unroll
  for (int s = 0; s < 2; ++s)
#pragma unroll
    for (int j = 0; j < 4; ++j) {
      pos[s][j] = pack_bf16(ep[8 * s + 2 * j] * rp, ep[8 * s + 2 * j + 1] * rp);
      neg[s][j] = pack_bf16(en[8 * s + 2 * j] * rn, en[8 * s + 2 * j + 1] * rn);
    }
}

constexpr int B_W    = 0;
constexpr int B_PHI  = 57344;
constexpr int B_PHIW = 32 * 144;
constexpr int B_RED  = 0;
constexpr int B_RED2 = B_PHI + 4 * B_PHIW;
constexpr int B_LDS  = 80 * 1024;

struct BState {
  u32x4 phq_pos[2], phq_neg[2];
};

DI void part_b1(char* sm, int h, int rt, const unsigned short* __restrict__ x,
                const unsigned short* __restrict__ wqkv, const unsigned short* __restrict__ wphi,
                float* __restrict__ kvpart, BState& st) {
  const int tid = threadIdx.x, lane = tid & 63, w = tid >> 6;
  const int c32 = lane & 31, hh = lane >> 5;
  const int off = (28 * h) & 7, c0 = 28 * h - off;
  {
    const int rsub = lane >> 3, p16 = lane & 7;
#pragma unroll
    for (int i = 0; i < 14; ++i) {
      const int chunk = w + 4 * i;
      const int row = 8 * chunk + rsub;
      const int seg = (p16 >> 2) ^ ((row >> 1) & 1);
      glds16(wqkv + (size_t)row * 1344 + 448 + 448 * seg + c0 + 8 * (p16 & 3), sm + B_W + chunk * 1024);
    }
  }
  bf16x8 xf[28];
  {
    const unsigned short* xp = x + (size_t)(128 * rt + 32 * w + c32) * D_ + 8 * hh;
#pragma unroll
    for (int t = 0; t < 28; ++t) xf[t] = *(const bf16x8*)(xp + 16 * t);
  }
  bf16x8 wph[2];
#pragma unroll
  for (int s = 0; s < 2; ++s)
#pragma unroll
    for (int j = 0; j < 8; ++j) {
      const int c = 16 * s + 8 * (j >> 2) + 4 * hh + (j & 3) - off;
      const bool ok = (c >= 0) && (c < 28) && (c32 < 28);
      const unsigned short v = wphi[h * 784 + (ok ? c * 28 + c32 : 0)];
      wph[s][j] = ok ? (short)v : (short)0;
    }
  wait_vm0();
  __syncthreads();
  const int q4 = (lane & 15) >> 2, p4 = lane & 3, cb = (lane >> 4) & 1;
  f32x16 accK, accV;
#pragma unroll
  for (int r = 0; r < 16; ++r) { accK[r] = 0.f; accV[r] = 0.f; }
  {
    const int sw = (q4 >> 1) & 1;
    const char* kb = sm + B_W + (8 * hh + q4) * 128 + (0 ^ sw) * 64 + 32 * cb + 8 * p4;
    const char* vb = sm + B_W + (8 * hh + q4) * 128 + (1 ^ sw) * 64 + 32 * cb + 8 * p4;
#pragma unroll
    for (int t = 0; t < 28; ++t) {
      const bf16x8 kf = tr_frag(kb + t * 2048, kb + t * 2048 + 512);
      const bf16x8 vf = tr_frag(vb + t * 2048, vb + t * 2048 + 512);
      accK = MFMA32(kf, xf[t], accK);
      accV = MFMA32(xf[t], vf, accV);
    }
  }
  __syncthreads();
  {
    const int rsub = lane >> 2, p16 = lane & 3;
#pragma unroll
    for (int i = 0; i < 7; ++i) {
      const int chunk = w + 4 * i;
      const int row = 16 * chunk + rsub;
      glds16(wqkv + (size_t)row * 1344 + c0 + 8 * p16, sm + B_W + chunk * 1024);
    }
  }
  f32x16 pk;
#pragma unroll
  for (int r = 0; r < 16; ++r) pk[r] = 0.f;
  pk = MFMA32(wph[0], pack_acc(accK, 0), pk);
  pk = MFMA32(wph[1], pack_acc(accK, 1), pk);
  u32x4 kpos[2], kneg[2];
  dual_softmax(pk, hh, kpos, kneg);
  char* phw = sm + B_PHI + w * B_PHIW;
#pragma unroll
  for (int gq = 0; gq < 4; ++gq) {
    u32x2 a = {kpos[gq >> 1][2 * (gq & 1)], kpos[gq >> 1][2 * (gq & 1) + 1]};
    u32x2 b = {kneg[gq >> 1][2 * (gq & 1)], kneg[gq >> 1][2 * (gq & 1) + 1]};
    *(u32x2*)(phw + c32 * 144 + (8 * gq + 4 * hh) * 2) = a;
    *(u32x2*)(phw + c32 * 144 + 64 + (8 * gq + 4 * hh) * 2) = b;
  }
  {
    const int cone = off ? 0 : 28;
    const bool valid = (c32 >= off) && (c32 < off + 28);
#pragma unroll
    for (int r = 0; r < 16; ++r) accV[r] = valid ? accV[r] : ((c32 == cone) ? 1.f : 0.f);
  }
  const bf16x8 vp0 = pack_acc(accV, 0), vp1 = pack_acc(accV, 1);
  asm volatile("s_waitcnt lgkmcnt(0)" ::: "memory");
  __builtin_amdgcn_wave_barrier();
  f32x16 kvp[2];
#pragma unroll
  for (int m = 0; m < 2; ++m) {
#pragma unroll
    for (int r = 0; r < 16; ++r) kvp[m][r] = 0.f;
    const char* pb = phw + (4 * hh + q4) * 144 + (32 * m + 16 * cb + 4 * p4) * 2;
    const bf16x8 a0 = tr_frag(pb, pb + 8 * 144);
    const bf16x8 a1 = tr_frag(pb + 16 * 144, pb + 24 * 144);
    kvp[m] = MFMA32(a0, vp0, kvp[m]);
    kvp[m] = MFMA32(a1, vp1, kvp[m]);
  }
  {
    char* rb = sm + B_W + 28672;
    if (w > 0) {
#pragma unroll
      for (int m = 0; m < 2; ++m)
#pragma unroll
        for (int gq = 0; gq < 4; ++gq) {
          f32x4 v = {kvp[m][4 * gq], kvp[m][4 * gq + 1], kvp[m][4 * gq + 2], kvp[m][4 * gq + 3]};
          *(f32x4*)(rb + (((w - 1) * 8 + m * 4 + gq) * 64 + lane) * 16) = v;
        }
    }
    asm volatile("s_waitcnt lgkmcnt(0)" ::: "memory");
    __builtin_amdgcn_s_barrier();
    if (w == 0) {
#pragma unroll
      for (int m = 0; m < 2; ++m)
#pragma unroll
        for (int gq = 0; gq < 4; ++gq) {
          f32x4 v = {kvp[m][4 * gq], kvp[m][4 * gq + 1], kvp[m][4 * gq + 2], kvp[m][4 * gq + 3]};
#pragma unroll
          for (int ww = 0; ww < 3; ++ww) v += *(const f32x4*)(rb + ((ww * 8 + m * 4 + gq) * 64 + lane) * 16);
          *(f32x4*)(kvpart + ((m * 4 + gq) * 64 + lane) * 4) = v;
        }
    }
  }
  wait_vm0();
  __syncthreads();
  f32x16 accQ;
#pragma unroll
  for (int r = 0; r < 16; ++r) accQ[r] = 0.f;
  {
    const char* qb = sm + B_W + (8 * hh + q4) * 64 + 32 * cb + 8 * p4;
#pragma unroll
    for (int t = 0; t < 28; ++t) {
      const bf16x8 qf = tr_frag(qb + t * 1024, qb + t * 1024 + 256);
      accQ = MFMA32(qf, xf[t], accQ);
    }
  }
  f32x16 pq;
#pragma unroll
  for (int r = 0; r < 16; ++r) pq[r] = 0.f;
  pq = MFMA32(wph[0], pack_acc(accQ, 0), pq);
  pq = MFMA32(wph[1], pack_acc(accQ, 1), pq);
  dual_softmax(pq, hh, st.phq_pos, st.phq_neg);
}

constexpr int B_KVL = 0;
DI void part_b15(char* sm, int h, int rt, const float* __restrict__ kvpart_h, const BState& st,
                 unsigned short* __restrict__ o) {
  const int tid = threadIdx.x, lane = tid & 63, w = tid >> 6;
  const int c32 = lane & 31, hh = lane >> 5;
  const int off = (28 * h) & 7;
#pragma unroll
  for (int i = 0; i < 2; ++i) {
    const int e = tid + 256 * i;
    f32x4 s = {0.f, 0.f, 0.f, 0.f};
#pragma unroll
    for (int r = 0; r < 16; ++r) s += *(const f32x4*)(kvpart_h + (size_t)r * 2048 + e * 4);
    const int m = e >> 8, gq = (e >> 6) & 3, ln = e & 63;
    const int f = 32 * m + 8 * gq + 4 * (ln >> 5), d = ln & 31;
    unsigned short* kl = (unsigned short*)(sm + B_KVL);
    const unsigned p0 = pack_bf16(s[0], s[1]), p1 = pack_bf16(s[2], s[3]);
    kl[(f + 0) * 32 + d] = (unsigned short)(p0 & 0xffffu);
    kl[(f + 1) * 32 + d] = (unsigned short)(p0 >> 16);
    kl[(f + 2) * 32 + d] = (unsigned short)(p1 & 0xffffu);
    kl[(f + 3) * 32 + d] = (unsigned short)(p1 >> 16);
  }
  __syncthreads();
  const int q4 = (lane & 15) >> 2, p4 = lane & 3, cb = (lane >> 4) & 1;
  f32x16 num;
#pragma unroll
  for (int r = 0; r < 16; ++r) num[r] = 0.f;
#pragma unroll
  for (int m = 0; m < 2; ++m)
#pragma unroll
    for (int s = 0; s < 2; ++s) {
      const char* pb = sm + B_KVL + (32 * m + 16 * s + 4 * hh + q4) * 64 + (16 * cb + 4 * p4) * 2;
      const bf16x8 a = tr_frag(pb, pb + 8 * 64);
      num = MFMA32(a, as_frag(m == 0 ? st.phq_pos[s] : st.phq_neg[s]), num);
    }
  const float dsrc = off ? num[0] : num[12];
  float den = __shfl(dsrc, c32 + (off ? 0 : 32));
  den = bfr(bfr(den) + 1e-6f);
  unsigned short* op = o + (size_t)(128 * rt + 32 * w + c32) * D_ + 28 * h;
#pragma unroll
  for (int gq = 0; gq < 4; ++gq) {
    const int d = 8 * gq + 4 * hh - off;
    if (d >= 0 && d < 28) {
      u32x2 v;
      v[0] = pack_bf16(bfr(num[4 * gq]) / den, bfr(num[4 * gq + 1]) / den);
      v[1] = pack_bf16(bfr(num[4 * gq + 2]) / den, bfr(num[4 * gq + 3]) / den);
      *(u32x2*)(op + d) = v;
    }
  }
}

constexpr int B_WO = 0;
DI void part_b2(char* sm, int rt2, int ct, const unsigned short* __restrict__ o,
                const unsigned short* __restrict__ wo, unsigned short* __restrict__ out) {
  const int tid = threadIdx.x, lane = tid & 63, w = tid >> 6;
  const int c32 = lane & 31, hh = lane >> 5;
  const int wr = w & 1, wc = w >> 1;
  for (int i = w; i < 49; i += 4) {
    const int id = i * 64 + lane;
    const int row = id / 7, c16 = id - 7 * row;
    glds16(wo + (size_t)row * D_ + 56 * ct + 8 * c16, sm + B_WO + i * 1024);
  }
  if (tid < 16) *(unsigned*)(sm + B_WO + 50176 + tid * 4) = 0u;
  bf16x8 of[28];
  {
    const unsigned short* op = o + (size_t)(64 * rt2 + 32 * wr + c32) * D_ + 8 * hh;
#pragma unroll
    for (int t = 0; t < 28; ++t) of[t] = *(const bf16x8*)(op + 16 * t);
  }
  wait_vm0();
  __syncthreads();
  const int q4 = (lane & 15) >> 2, p4 = lane & 3, cb = (lane >> 4) & 1;
  f32x16 acc;
#pragma unroll
  for (int r = 0; r < 16; ++r) acc[r] = 0.f;
  const char* wb = sm + B_WO + (8 * hh + q4) * 112 + (28 * wc + 16 * cb + 4 * p4) * 2;
#pragma unroll
  for (int t = 0; t < 28; ++t) {
    const bf16x8 a = tr_frag(wb + t * 1792, wb + t * 1792 + 448);
    acc = MFMA32(a, of[t], acc);
  }
  unsigned short* dp = out + (size_t)(64 * rt2 + 32 * wr + c32) * OUTW + 448 + 56 * ct + 28 * wc;
#pragma unroll
  for (int gq = 0; gq < 4; ++gq) {
    const int c = 8 * gq + 4 * hh;
    if (c < 28) {
      u32x2 v;
      v[0] = pack_bf16(acc[4 * gq], acc[4 * gq + 1]);
      v[1] = pack_bf16(acc[4 * gq + 2], acc[4 * gq + 3]);
      *(u32x2*)(dp + c) = v;
    }
  }
}

struct Params {
  const unsigned short *u, *logA, *Aim, *Cre, *Cim, *logdt, *Dsk, *x, *wqkv, *wphi, *wo;
  unsigned short* out;
  float* kvpart;
  unsigned short* o;
  unsigned* phq;
};

__global__ void __launch_bounds__(256, 2) k1(Params p) {
  extern __shared__ __attribute__((aligned(16))) char sm[];
  const int b = blockIdx.x;
  if (b < 256) {
    const int h = b >> 4, rt = b & 15;
    BState st;
    part_b1(sm, h, rt, p.x, p.wqkv, p.wphi, p.kvpart + (size_t)(h * 16 + rt) * 2048, st);
    unsigned* dst = p.phq + (size_t)b * 4096 + threadIdx.x;
#pragma unroll
    for (int s = 0; s < 2; ++s)
#pragma unroll
      for (int j = 0; j < 4; ++j) {
        dst[(s * 4 + j) * 256] = st.phq_pos[s][j];
        dst[(8 + s * 4 + j) * 256] = st.phq_neg[s][j];
      }
  } else {
    const int a = b - 256;
    part_a_block(sm, 2 * a, p.u, p.logA, p.Aim, p.Cre, p.Cim, p.logdt, p.Dsk, p.out);
  }
}
__global__ void __launch_bounds__(256, 2) k2(Params p) {
  extern __shared__ __attribute__((aligned(16))) char sm[];
  const int b = blockIdx.x, h = b >> 4, rt = b & 15;
  BState st;
  const unsigned* src = p.phq + (size_t)b * 4096 + threadIdx.x;
#pragma unroll
  for (int s = 0; s < 2; ++s)
#pragma unroll
    for (int j = 0; j < 4; ++j) {
      st.phq_pos[s][j] = src[(s * 4 + j) * 256];
      st.phq_neg[s][j] = src[(8 + s * 4 + j) * 256];
    }
  part_b15(sm, h, rt, p.kvpart + (size_t)h * 16 * 2048, st, p.o);
}
__global__ void __launch_bounds__(256, 2) k3(Params p) {
  extern __shared__ __attribute__((aligned(16))) char sm[];
  const int b = blockIdx.x;
  part_b2(sm, b >> 3, b & 7, p.o, p.wo, p.out);
}

extern "C" void kernel_launch(void* const* d_in, const int* in_sizes, int n_in,
                              void* d_out, int out_size, void* d_ws, size_t ws_size,
                              hipStream_t stream) {
  Params p;
  p.u = (const unsigned short*)d_in[0];
  p.logA = (const unsigned short*)d_in[1];
  p.Aim = (const unsigned short*)d_in[2];
  p.Cre = (const unsigned short*)d_in[3];
  p.Cim = (const unsigned short*)d_in[4];
  p.logdt = (const unsigned short*)d_in[5];
  p.Dsk = (const unsigned short*)d_in[6];
  p.x = (const unsigned short*)d_in[7];
  p.wqkv = (const unsigned short*)d_in[8];
  p.wphi = (const unsigned short*)d_in[9];
  p.wo = (const unsigned short*)d_in[10];
  p.out = (unsigned short*)d_out;
  char* ws = (char*)d_ws;
  p.kvpart = (float*)(ws + (1 << 20));
  p.o = (unsigned short*)(ws + (4 << 20));
  p.phq = (unsigned*)(ws + (8 << 20));
  static_assert(A_LDS <= B_LDS, "lds");
  hipFuncSetAttribute((const void*)k1, hipFuncAttributeMaxDynamicSharedMemorySize, B_LDS);
  hipFuncSetAttribute((const void*)k3, hipFuncAttributeMaxDynamicSharedMemorySize, 52 * 1024);
  k1<<<480, 256, B_LDS, stream>>>(p);
  k2<<<256, 256, 4096, stream>>>(p);
  k3<<<256, 256, 52 * 1024, stream>>>(p);
}
```

```cpp
#include <hip/hip_runtime.h>
#include <hip/hip_bf16.h>
#include <stdint.h>

using bf16x8 = __attribute__((ext_vector_type(8))) short;
using s16x4  = __attribute__((ext_vector_type(4))) short;
using f32x16 = __attribute__((ext_vector_type(16))) float;
using f32x4  = __attribute__((ext_vector_type(4))) float;
using u32x4  = __attribute__((ext_vector_type(4))) unsigned;
using u32x2  = __attribute__((ext_vector_type(2))) unsigned;
typedef __bf16 bfx2_t __attribute__((ext_vector_type(2)));
typedef float  f32x2  __attribute__((ext_vector_type(2)));
typedef unsigned long long u64;

#define DI __device__ __forceinline__
#define LAS __attribute__((address_space(3)))
#define GAS __attribute__((address_space(1)))
#define MFMA32(a, b, c) __builtin_amdgcn_mfma_f32_32x32x16_bf16((a), (b), (c), 0, 0, 0)
#define MFMAF(a, b, c) __builtin_amdgcn_mfma_f32_16x16x4f32((a), (b), (c), 0, 0, 0)
#define MFMA16(a, b, c) __builtin_amdgcn_mfma_f32_16x16x32_bf16((a), (b), (c), 0, 0, 0)

constexpr int D_  = 448;
constexpr int N_  = 256;
constexpr int OUTW = 896;

#define STAMP(i)
#define WSTAMP(i)

constexpr unsigned WS_EB  = 0;
constexpr unsigned WS_EA  = 256;
constexpr unsigned WS_FKV = 4096;
constexpr unsigned WS_FO  = 8192;
constexpr unsigned WS_FUT = 12288;
constexpr unsigned WS_KVP = 1u << 20;
constexpr unsigned WS_O   = 4u << 20;
constexpr unsigned WS_UT  = 8u << 20;
constexpr unsigned WS_END = 10u << 20;

DI unsigned pack_bf16(float lo, float hi) {
  f32x2 v = {lo, hi};
  return __builtin_bit_cast(unsigned, __builtin_convertvector(v, bfx2_t));
}
DI float bf_lo(unsigned p) { return __uint_as_float(p << 16); }
DI float bf_hi(unsigned p) { return __uint_as_float(p & 0xffff0000u); }
DI float bf2f(unsigned short b) { return __uint_as_float(((unsigned)b) << 16); }
DI float bfr(float x) { return bf_lo(pack_bf16(x, 0.f)); }
DI s16x4 tr_read(const char* p) {
  return __builtin_amdgcn_ds_read_tr16_b64_v4i16((LAS s16x4*)(p));
}
DI bf16x8 tr_frag(const char* p0, const char* p1) {
  s16x4 a = tr_read(p0), b = tr_read(p1);
  return __builtin_shufflevector(a, b, 0, 1, 2, 3, 4, 5, 6, 7);
}
DI bf16x8 as_frag(u32x4 v) { return __builtin_bit_cast(bf16x8, v); }
DI void glds16(const void* g, void* lds_wave_base) {
  __builtin_amdgcn_global_load_lds((const unsigned*)g, (unsigned*)lds_wave_base, 16, 0, 0);
}
DI void wait_vm0() { asm volatile("s_waitcnt vmcnt(0)" ::: "memory"); }
DI void bar_lds() { asm volatile("s_waitcnt lgkmcnt(0)" ::: "memory"); __builtin_amdgcn_s_barrier(); asm volatile("" ::: "memory"); }

typedef __amdgpu_buffer_rsrc_t rsrc_t;
DI void st16_wt(rsrc_t r, unsigned off, u32x4 v) { __builtin_amdgcn_raw_buffer_store_b128(v, r, off, 0, 16); }
DI void st8_wt(rsrc_t r, unsigned off, u32x2 v) { __builtin_amdgcn_raw_buffer_store_b64(v, r, off, 0, 16); }
DI u32x4 ld16_wt(rsrc_t r, unsigned off) { return __builtin_amdgcn_raw_buffer_load_b128(r, off, 0, 16); }
DI unsigned ld4_wt(rsrc_t r, unsigned off) { return __builtin_amdgcn_raw_buffer_load_b32(r, off, 0, 16); }
DI u64 ld_flag(const char* ws, unsigned off) {
  return __hip_atomic_load((GAS u64*)(ws + off), __ATOMIC_RELAXED, __HIP_MEMORY_SCOPE_AGENT);
}
DI void st_flag(char* ws, unsigned off, u64 v) {
  __hip_atomic_store((GAS u64*)(ws + off), v, __ATOMIC_RELAXED, __HIP_MEMORY_SCOPE_AGENT);
}
DI void wait_flags(const char* ws, unsigned off, int n, u64 tag) {
  const int lane = threadIdx.x & 63;
  for (int it = 0; it < (1 << 16); ++it) {
    const u64 v = (lane < n) ? ld_flag(ws, off + 8 * lane) : tag;
    if (__all(v == tag)) break;
    __builtin_amdgcn_s_sleep(1);
  }
  asm volatile("" ::: "memory");
}
DI u64 uniform64(u64 v) {
  const unsigned lo = __builtin_amdgcn_readfirstlane((unsigned)v), hi = __builtin_amdgcn_readfirstlane((unsigned)(v >> 32));
  return ((u64)hi << 32) | lo;
}

struct Params {
  const unsigned short *u, *logA, *Aim, *Cre, *Cim, *logdt, *Dsk, *x, *wqkv, *wphi, *wo;
  unsigned short* out;
  char* ws;
};

constexpr int LDS_BYTES = 80 * 1024;

constexpr int A_UCOPY = 4160;
constexpr int A_US    = 0;
constexpr int A_CH0   = 4 * A_UCOPY;
constexpr int A_KRH   = 0;
constexpr int A_KRL   = 4576;
constexpr int A_KD    = 9152;
constexpr int A_PR2   = A_KD + 4112;
constexpr int A_RED   = A_PR2 + 2048;
constexpr int A_SUM   = A_RED + 16384;
constexpr int A_CHS   = A_SUM + 64;
static_assert(A_CH0 + 2 * A_CHS <= LDS_BYTES, "A lds");

DI void cexp_eval(float lr, float li, float t, float& er, float& ei) {
  const float xr = lr * t;
  const float e  = __builtin_amdgcn_exp2f(xr * 1.44269504088896340736f);
  const float x  = li * t;
  const float C_HI = 0.15915494309189533577f;
  const float C_LO = (float)(0.15915494309189533577 - (double)0.15915494309189533577f);
  const float rh = x * C_HI;
  float rl = __builtin_fmaf(x, C_HI, -rh);
  rl = __builtin_fmaf(x, C_LO, rl);
  const float rev = (rh - __builtin_rintf(rh)) + rl;
  er = e * __builtin_amdgcn_cosf(rev);
  ei = e * __builtin_amdgcn_sinf(rev);
}
DI void cexp_fast(const f32x4& p, float t, float& er, float& ei) {
  const float e = __builtin_amdgcn_exp2f(p[0] * t);
  const float rev = __builtin_fmaf(t, p[2], __builtin_amdgcn_fractf(t * p[1]));
  er = e * __builtin_amdgcn_cosf(rev);
  ei = e * __builtin_amdgcn_sinf(rev);
}
DI void split2(float a, float b, unsigned& hi, unsigned& lo) {
  hi = pack_bf16(a, b);
  lo = pack_bf16(a - bf_lo(hi), b - bf_hi(hi));
}
DI void split2v(const f32x2& R, const f32x2& I, u32x4& h, u32x4& l, int q) {
  const unsigned h0 = pack_bf16(R[0], I[0]), h1 = pack_bf16(R[1], I[1]);
  const f32x2 rl = R - f32x2{bf_lo(h0), bf_lo(h1)};
  const f32x2 il = I - f32x2{bf_hi(h0), bf_hi(h1)};
  h[2 * q] = h0; h[2 * q + 1] = h1;
  l[2 * q] = pack_bf16(rl[0], il[0]); l[2 * q + 1] = pack_bf16(rl[1], il[1]);
}
DI int kd_index(int tap) {
  unsigned u = __float_as_uint((float)tap);
  u = (u + 0x7fffu + ((u >> 16) & 1u)) & 0xffff0000u;
  const int p = (int)__uint_as_float(u);
  if (p < 256) return p;
  if (p >= 2048) return 1024;
  const int s = (31 - __builtin_clz(p)) - 7;
  return s * 256 + (p >> s);
}

DI void part_a(char* sm, const Params& P, int a) {
  const int tid = threadIdx.x, lane = tid & 63, w = __builtin_amdgcn_readfirstlane(tid >> 6);
  const int ch = w >> 2, cw = w & 3, ctid = tid & 255;
  const int i16 = lane & 15, g = lane >> 4;
  const int d0 = 56 * (a & 7) + 2 * (a >> 3);
  const int cg = d0 >> 6, r32 = (d0 & 63) >> 1;
  const int d = d0 + ch;
  char* cs = sm + A_CH0 + ch * A_CHS;
  const rsrc_t wsr = __builtin_amdgcn_make_buffer_rsrc(P.ws, 0, WS_END, 0x00020000);
  STAMP(0);
  const u64 ep = ld_flag(P.ws, WS_EA + 256 * cg);
  const u32x4 ut = *(const u32x4*)(P.u + (size_t)(64 * r32 + (tid >> 3)) * D_ + 64 * cg + 8 * (tid & 7));
  const unsigned short pla = P.logA[d * N_ + ctid], pai = P.Aim[d * N_ + ctid];
  const unsigned short pcr = P.Cre[d * N_ + ctid], pci = P.Cim[d * N_ + ctid];
  const unsigned short pdt = P.logdt[d];
  {
    unsigned short* T = (unsigned short*)(sm + A_CH0 + A_RED);
    const int row = tid >> 3, c8 = 8 * (tid & 7);
#pragma unroll
    for (int j = 0; j < 4; ++j) {
      T[(c8 + 2 * j) * 72 + row] = (unsigned short)(ut[j] & 0xffffu);
      T[(c8 + 2 * j + 1) * 72 + row] = (unsigned short)(ut[j] >> 16);
    }
    if (tid < 64) {
      const int c = tid >> 4, e = tid & 15;
      ((unsigned short*)(sm + A_US + c * A_UCOPY))[e] = 0;
    }
    bar_lds();
    const u32x4 v = *(const u32x4*)(T + (tid >> 3) * 72 + 8 * (tid & 7));
    st16_wt(wsr, WS_UT + ((64 * cg + (tid >> 3)) * 2048 + 64 * r32 + 8 * (tid & 7)) * 2, v);
  }
  {
    const float dt = bfr(expf(bf2f(pdt)));
    const float am = bfr(expf(bf2f(pla)));
    const float lr = -am * dt, li = bf2f(pai) * dt;
    const float cr = bf2f(pcr) * dt, ci = bf2f(pci) * dt;
    {
      const float C_HI = 0.15915494309189533577f;
      const float C_LO = (float)(0.15915494309189533577 - (double)0.15915494309189533577f);
      const float q = li * C_HI;
      const float er = __builtin_fmaf(li, C_LO, __builtin_fmaf(li, C_HI, -q));
      const float a16 = __uint_as_float(__float_as_uint(q) & 0xffffff00u);
      *(f32x4*)(cs + A_KD + ctid * 16) = f32x4{lr * 1.44269504088896340736f, a16, (q - a16) + er, 0.f};
      *(f32x2*)(cs + A_PR2 + ctid * 8) = f32x2{cr, ci};
    }
    float er, ei;
    cexp_eval(lr, li, 2048.f, er, ei);
    float t = cr * er - ci * ei;
#pragma unroll
    for (int o = 32; o >= 1; o >>= 1) t += __shfl_xor(t, o);
    if (lane == 0) ((float*)(cs + A_SUM))[cw] = t;
  }
  wait_vm0();
  const u64 tag = uniform64(ep) + 1ull;
  bar_lds();
  if (tid == 0) st_flag(P.ws, WS_FUT + 256 * cg + 8 * r32, tag);
  STAMP(1);
  {
    f32x4 acc[4];
#pragma unroll
    for (int s = 0; s < 4; ++s) acc[s] = f32x4{0.f, 0.f, 0.f, 0.f};
    const float ty = (float)i16, tx = (float)(16 * i16);
    WSTAMP(0);
#pragma unroll 1
    for (int ks = 0; ks < 4; ++ks) {
      if (ks == 1) WSTAMP(1);
      f32x2 YR[2], YI[2], XR[2], XI[2], CR[2], CI[2];
      const char* pp = cs + A_KD + (64 * cw + 16 * ks + 4 * g) * 16;
      const char* pc = cs + A_PR2 + (64 * cw + 16 * ks + 4 * g) * 8;
#pragma unroll
      for (int m = 0; m < 4; ++m) {
        const f32x4 p = *(const f32x4*)(pp + m * 16);
        float r0, i0, r1, i1;
        cexp_fast(p, ty, r0, i0);
        cexp_fast(p, tx, r1, i1);
        YR[m >> 1][m & 1] = r0; YI[m >> 1][m & 1] = i0;
        XR[m >> 1][m & 1] = r1; XI[m >> 1][m & 1] = i1;
      }
#pragma unroll
      for (int q = 0; q < 2; ++q) {
        const f32x4 c = *(const f32x4*)(pc + q * 16);
        CR[q] = f32x2{c[0], c[2]}; CI[q] = f32x2{c[1], c[3]};
      }
#pragma unroll
      for (int s = 0; s < 4; ++s) {
        u32x4 ah, al, bh, bl;
#pragma unroll
        for (int q = 0; q < 2; ++q) {
          const f32x2 zr = CR[q] * XR[q] - CI[q] * XI[q];
          const f32x2 zn = -(CR[q] * XI[q]) - CI[q] * XR[q];
          split2v(YR[q], YI[q], ah, al, q);
          split2v(zr, zn, bh, bl, q);
        }
        acc[s] = MFMA16(as_frag(ah), as_frag(bh), acc[s]);
        acc[s] = MFMA16(as_frag(ah), as_frag(bl), acc[s]);
        acc[s] = MFMA16(as_frag(al), as_frag(bh), acc[s]);
        if (s < 3) {
#pragma unroll
          for (int q = 0; q < 2; ++q) {
            const f32x2 a = YR[q], b = YI[q], c = XR[q], e = XI[q];
            YR[q] = a * a - b * b; YI[q] = (a + a) * b;
            XR[q] = c * c - e * e; XI[q] = (c + c) * e;
          }
        }
      }
    }
    WSTAMP(2);
#pragma unroll
    for (int s = 0; s < 4; ++s)
      *(f32x4*)(cs + A_RED + ((cw * 4 + s) * 256 + 16 * i16 + 4 * g) * 4) = acc[s];
  }
  bar_lds();
  STAMP(2);
  if (w == 0) wait_flags(P.ws, WS_FUT + 256 * cg, 32, tag);
  {
    const float* red = (const float*)(cs + A_RED);
    float* kd = (float*)(cs + A_KD);
#pragma unroll
    for (int s = 0; s < 4; ++s)
      kd[s * 256 + ctid] = 2.f * ((red[(0 * 4 + s) * 256 + ctid] + red[(1 * 4 + s) * 256 + ctid]) +
                                  (red[(2 * 4 + s) * 256 + ctid] + red[(3 * 4 + s) * 256 + ctid]));
    if (ctid == 0) {
      const float* sp = (const float*)(cs + A_SUM);
      kd[1024] = 2.f * ((sp[0] + sp[1]) + (sp[2] + sp[3]));
    }
  }
  bar_lds();
  const u32x4 uv = ld16_wt(wsr, WS_UT + (d * 2048 + 8 * ctid) * 2);
  const unsigned unx = (ctid < 255) ? ld4_wt(wsr, WS_UT + (d * 2048 + 8 * ctid + 8) * 2) : 0u;
  {
    const float* kd = (const float*)(cs + A_KD);
    for (int c = ctid; c < 286; c += 256) {
      float v[8];
#pragma unroll
      for (int j = 0; j < 8; ++j) {
        const int tap = 2047 - (8 * c + j);
        v[j] = (tap >= 0) ? kd[kd_index(tap < 0 ? 0 : tap)] : 0.f;
      }
      u32x4 h, l;
#pragma unroll
      for (int j = 0; j < 4; ++j) { unsigned p, q; split2(v[2 * j], v[2 * j + 1], p, q); h[j] = p; l[j] = q; }
      *(u32x4*)(cs + A_KRH + c * 16) = h;
      *(u32x4*)(cs + A_KRL + c * 16) = l;
    }
  }
  {
    char* c0p = sm + A_US + (2 * ch) * A_UCOPY;
    char* c1p = c0p + A_UCOPY;
    *(u32x4*)(c0p + (8 * ctid + 16) * 2) = uv;
    u32x4 sh;
    sh[0] = (uv[0] >> 16) | (uv[1] << 16); sh[1] = (uv[1] >> 16) | (uv[2] << 16);
    sh[2] = (uv[2] >> 16) | (uv[3] << 16); sh[3] = (uv[3] >> 16) | (unx << 16);
    *(u32x4*)(c1p + (8 * ctid + 16) * 2) = sh;
    if (ctid == 0) ((unsigned short*)c1p)[15] = (unsigned short)(uv[0] & 0xffffu);
  }
  bar_lds();
  STAMP(3);
  f32x4 acc[8];
  {
#pragma unroll
    for (int q = 0; q < 8; ++q) acc[q] = f32x4{0.f, 0.f, 0.f, 0.f};
    const int par = (i16 + 1) & 1;
    const char* ubase = sm + A_US + (2 * ch + par) * A_UCOPY + 2 * (i16 + 1 - par + 8 * g);
#pragma unroll 1
    for (int cc = 0; cc < 2; ++cc) {
      const int r = 2 * cw + cc;
      bf16x8 af[8];
#pragma unroll
      for (int q = 0; q < 8; ++q) {
        const unsigned* p = (const unsigned*)(ubase + 64 * (r + 8 * q));
        u32x4 v; v[0] = p[0]; v[1] = p[1]; v[2] = p[2]; v[3] = p[3];
        af[q] = as_frag(v);
      }
      const int kb = 2 * (2032 - 16 * i16 + 32 * r + 8 * g);
#pragma unroll
      for (int b = 0; b < 8; ++b) {
        const bf16x8 bh = *(const bf16x8*)(cs + A_KRH + kb - 512 * b);
        const bf16x8 bl = *(const bf16x8*)(cs + A_KRL + kb - 512 * b);
#pragma unroll
        for (int q = 0; q + b < 8; ++q) acc[q + b] = MFMA16(af[q], bh, acc[q + b]);
#pragma unroll
        for (int q = 0; q + b < 8; ++q) acc[q + b] = MFMA16(af[q], bl, acc[q + b]);
      }
    }
  }
  STAMP(4);
  const unsigned short pds = P.Dsk[d];
  f32x4 y2[2];
  {
    const bool up = (cw & 2) != 0;
    f32x4 keep[4];
#pragma unroll
    for (int i = 0; i < 4; ++i) {
      keep[i] = up ? acc[4 + i] : acc[i];
      const f32x4 snd = up ? acc[i] : acc[4 + i];
      *(f32x4*)(cs + A_RED + ((cw * 4 + i) * 64 + lane) * 16) = snd;
    }
    bar_lds();
#pragma unroll
    for (int i = 0; i < 4; ++i) keep[i] += *(const f32x4*)(cs + A_RED + (((cw ^ 2) * 4 + i) * 64 + lane) * 16);
    const bool od = (cw & 1) != 0;
#pragma unroll
    for (int i = 0; i < 2; ++i) {
      y2[i] = od ? keep[2 + i] : keep[i];
      const f32x4 snd = od ? keep[i] : keep[2 + i];
      *(f32x4*)(cs + A_KRH + ((cw * 2 + i) * 64 + lane) * 16) = snd;
    }
    bar_lds();
#pragma unroll
    for (int i = 0; i < 2; ++i) y2[i] += *(const f32x4*)(cs + A_KRH + (((cw ^ 1) * 2 + i) * 64 + lane) * 16);
  }
  {
    const float ds = bf2f(pds);
    const unsigned short* uc = (const unsigned short*)(sm + A_US + (2 * ch) * A_UCOPY) + 16;
#pragma unroll
    for (int i = 0; i < 2; ++i) {
      const int l = 256 * (4 * (cw >> 1) + 2 * (cw & 1) + i) + 16 * i16 + 4 * g;
      const u32x2 uu = *(const u32x2*)(uc + l);
      u32x2 o;
      o[0] = pack_bf16(bfr(y2[i][0]) + bfr(bf_lo(uu[0]) * ds), bfr(y2[i][1]) + bfr(bf_hi(uu[0]) * ds));
      o[1] = pack_bf16(bfr(y2[i][2]) + bfr(bf_lo(uu[1]) * ds), bfr(y2[i][3]) + bfr(bf_hi(uu[1]) * ds));
      *(u32x2*)(cs + A_KD + l * 2) = o;
    }
  }
  bar_lds();
  {
    const unsigned short* o0 = (const unsigned short*)(sm + A_CH0 + A_KD);
    const unsigned short* o1 = (const unsigned short*)(sm + A_CH0 + A_CHS + A_KD);
#pragma unroll
    for (int i = 0; i < 4; ++i) {
      const int l = tid + 512 * i;
      *(unsigned*)(P.out + (size_t)l * OUTW + d0) = (unsigned)o0[l] | ((unsigned)o1[l] << 16);
    }
  }
  if (r32 == 0 && tid == 0) st_flag(P.ws, WS_EA + 256 * cg, tag);
  STAMP(5);
}

DI bf16x8 pack_acc(const f32x16& x, int s) {
  u32x4 p;
#pragma unroll
  for (int j = 0; j < 4; ++j) p[j] = pack_bf16(x[8 * s + 2 * j], x[8 * s + 2 * j + 1]);
  return as_frag(p);
}

DI void dual_softmax(const f32x16& pacc, int hh, u32x4 (&pos)[2], u32x4 (&neg)[2]) {
  float p[16];
#pragma unroll
  for (int r = 0; r < 16; ++r) p[r] = bfr(pacc[r]);
  float mx = p[0], mn = p[0];
#pragma unroll
  for (int r = 1; r < 12; ++r) { mx = fmaxf(mx, p[r]); mn = fminf(mn, p[r]); }
#pragma unroll
  for (int r = 12; r < 16; ++r) { mx = hh ? mx : fmaxf(mx, p[r]); mn = hh ? mn : fminf(mn, p[r]); }
  mx = fmaxf(mx, __shfl_xor(mx, 32));
  mn = fminf(mn, __shfl_xor(mn, 32));
  float ep[16], en[16], sp = 0.f, sn = 0.f;
#pragma unroll
  for (int r = 0; r < 16; ++r) {
    const bool valid = (r < 12) || (hh == 0);
    ep[r] = valid ? __expf(p[r] - mx) : 0.f;
    en[r] = valid ? __expf(mn - p[r]) : 0.f;
    sp += ep[r]; sn += en[r];
  }
  sp += __shfl_xor(sp, 32);
  sn += __shfl_xor(sn, 32);
  const float rp = 1.f / sp, rn = 1.f / sn;
#pragma unroll
  for (int s = 0; s < 2; ++s)
#pragma unroll
    for (int j = 0; j < 4; ++j) {
      pos[s][j] = pack_bf16(ep[8 * s + 2 * j] * rp, ep[8 * s + 2 * j + 1] * rp);
      neg[s][j] = pack_bf16(en[8 * s + 2 * j] * rn, en[8 * s + 2 * j + 1] * rn);
    }
}
DI void acc_store(char* p, const f32x16& a, int lane) {
#pragma unroll
  for (int gq = 0; gq < 4; ++gq)
    *(f32x4*)(p + (gq * 64 + lane) * 16) = f32x4{a[4 * gq], a[4 * gq + 1], a[4 * gq + 2], a[4 * gq + 3]};
}
DI void acc_add(const char* p, f32x16& a, int lane) {
#pragma unroll
  for (int gq = 0; gq < 4; ++gq) {
    const f32x4 v = *(const f32x4*)(p + (gq * 64 + lane) * 16);
    a[4 * gq] += v[0]; a[4 * gq + 1] += v[1]; a[4 * gq + 2] += v[2]; a[4 * gq + 3] += v[3];
  }
}

constexpr int B_W    = 0;
constexpr int B_COMB = 28672;
constexpr int B_PHI  = 61440;
constexpr int B_PHIW = 32 * 136;
constexpr int B_KVL  = 61440;
constexpr int B_WPH  = 79872;
constexpr int B_XS   = 57344;
constexpr int B_CNT  = 79360;
constexpr int B_CMB2 = 53248;

DI void part_b(char* sm, const Params& P, int b) {
  const int tid = threadIdx.x, lane = tid & 63, w = __builtin_amdgcn_readfirstlane(tid >> 6);
  const int wr = w & 3, kh = w >> 2;
  const int c32 = lane & 31, hh = lane >> 5;
  const int xcd = b & 7, jj = b >> 3;
  const int rt = 4 * (xcd & 3) + (jj & 3), h = 8 * (xcd >> 2) + (jj >> 2);
  const int off = (28 * h) & 7, c0 = 28 * h - off;
  const int q4 = (lane & 15) >> 2, p4 = lane & 3, cb = (lane >> 4) & 1;
  const rsrc_t wsr = __builtin_amdgcn_make_buffer_rsrc(P.ws, 0, WS_END, 0x00020000);
  STAMP(0);
  const u64 ep = ld_flag(P.ws, WS_EB);
  {
    const int rsub = lane >> 3, p16 = lane & 7;
#pragma unroll
    for (int i = 0; i < 7; ++i) {
      int chunk = w + 8 * i + 14 * (jj & 3);
      chunk = chunk >= 56 ? chunk - 56 : chunk;
      const int row = 8 * chunk + rsub;
      const int seg = (p16 >> 2) ^ ((row >> 1) & 1);
      glds16(P.wqkv + (size_t)row * 1344 + 448 + 448 * seg + c0 + 8 * (p16 & 3), sm + B_W + chunk * 1024);
    }
  }
  bf16x8 xf[16];
  int lrot[4];
#pragma unroll
  for (int L = 0; L < 4; ++L) lrot[L] = kh ? (L + (jj >> 2)) % 3 : ((L + (jj >> 2)) & 3);
  const GAS bf16x8* xp = (const GAS bf16x8*)(P.x + (size_t)(128 * rt + 32 * wr + c32) * D_ + 8 * hh);
  {
    u32x4 raw[16];
    const GAS u32x4* xr = (const GAS u32x4*)(P.x + (size_t)(128 * rt + 32 * wr + (lane >> 3)) * D_ + 256 * kh + 8 * (lane & 7));
#pragma unroll
    for (int i = 0; i < 16; ++i)
      if (i < 12 || kh == 0) raw[i] = xr[(i & 3) * 8 * 56 + lrot[i >> 2] * 8];
    char* xs = sm + B_XS + w * 2048;
    const int cq = lane & 3, hsel = (lane >> 2) & 1;
    const int rsw = (c32 >> 2) & 3;
#pragma unroll
    for (int L = 0; L < 4; ++L) {
      if (L < 3 || kh == 0) {
#pragma unroll
        for (int hf = 0; hf < 2; ++hf) {
          if (hsel == hf) {
#pragma unroll
            for (int rg = 0; rg < 4; ++rg) {
              const int row = 8 * rg + (lane >> 3);
              *(u32x4*)(xs + row * 64 + ((cq ^ ((row >> 2) & 3)) * 16)) = raw[4 * L + rg];
            }
          }
          __builtin_amdgcn_fence(__ATOMIC_RELEASE, "wavefront"); __builtin_amdgcn_wave_barrier(); asm volatile("" ::: "memory");
#pragma unroll
          for (int tq = 0; tq < 2; ++tq)
            xf[4 * L + 2 * hf + tq] = *(const bf16x8*)(xs + c32 * 64 + (((2 * tq + hh) ^ rsw) * 16));
          asm volatile("" ::: "memory"); __builtin_amdgcn_wave_barrier();
        }
      }
    }
  }
  {
    const int c = (tid >> 4) - off, f0 = 2 * (tid & 15);
    const bool okc = (c >= 0) && (c < 28) && (f0 < 28);
    const unsigned short* wp = P.wphi + h * 784 + (okc ? c * 28 + f0 : 0);
    const unsigned v = (unsigned)wp[0] | ((unsigned)wp[1] << 16);
    *(unsigned*)(sm + B_WPH + tid * 4) = okc ? v : 0u;
    if (tid == 0) *(unsigned*)(sm + B_CNT) = 0u;
  }
  wait_vm0();
  const u64 tag = uniform64(ep) + 1ull;
  bar_lds();
  STAMP(1);
  f32x16 accK, accV;
#pragma unroll
  for (int r = 0; r < 16; ++r) { accK[r] = 0.f; accV[r] = 0.f; }
  {
    const int sw = (q4 >> 1) & 1;
    const char* kb = sm + B_W + (256 * kh + 8 * hh + q4) * 128 + (0 ^ sw) * 64 + 32 * cb + 8 * p4;
    const int vo = (sw ? -64 : 64);
    bf16x8 kf[2], vf[2];
    { const char* a = kb + lrot[0] * 8192; kf[0] = tr_frag(a, a + 512); vf[0] = tr_frag(a + vo, a + vo + 512); }
#pragma unroll
    for (int t = 0; t < 16; ++t) {
      if (t < 12 || kh == 0) {
        if (t + 1 < 12 || (t + 1 < 16 && kh == 0)) {
          const char* a = kb + lrot[(t + 1) >> 2] * 8192 + ((t + 1) & 3) * 2048;
          kf[(t + 1) & 1] = tr_frag(a, a + 512);
          vf[(t + 1) & 1] = tr_frag(a + vo, a + vo + 512);
        }
        accK = MFMA32(kf[t & 1], xf[t], accK);
        accV = MFMA32(xf[t], vf[t & 1], accV);
      }
      __builtin_amdgcn_sched_barrier(0);
    }
  }
  bar_lds();
  STAMP(2);
  const char* wpb = sm + B_WPH + (4 * hh + q4) * 64 + (16 * cb + 4 * p4) * 2;
  char* cslot = sm + B_COMB + wr * 8192;
  u32x4 phq_pos[2], phq_neg[2];
  if (kh == 1) {
    {
      const int rsub = lane >> 2, p16 = lane & 3;
#pragma unroll
      for (int i = 0; i < 7; ++i) {
        const int chunk = wr + 4 * i;
        glds16(P.wqkv + (size_t)(16 * chunk + rsub) * 1344 + c0 + 8 * p16, sm + B_W + chunk * 1024);
      }
    }
    acc_store(cslot, accK, lane); acc_store(cslot + 4096, accV, lane);
    bar_lds();
    __builtin_amdgcn_sched_barrier(0);
    const GAS bf16x8* xq = xp;
    asm volatile("" : "+v"(xq));
    bf16x8 xa[14], xb[14];
#pragma unroll
    for (int t = 0; t < 14; ++t) xa[t] = xq[2 * t];
    wait_vm0();
    bar_lds();
    f32x16 accQ;
#pragma unroll
    for (int r = 0; r < 16; ++r) accQ[r] = 0.f;
    const char* qb = sm + B_W + (8 * hh + q4) * 64 + 32 * cb + 8 * p4;
    bf16x8 qf[3];
    qf[0] = tr_frag(qb, qb + 256); qf[1] = tr_frag(qb + 1024, qb + 1024 + 256);
#pragma unroll
    for (int t = 0; t < 28; ++t) {
      if (t + 2 < 28) qf[(t + 2) % 3] = tr_frag(qb + (t + 2) * 1024, qb + (t + 2) * 1024 + 256);
      accQ = MFMA32(qf[t % 3], (t < 14) ? xa[t < 14 ? t : 0] : xb[t >= 14 ? t - 14 : 0], accQ);
      if (t == 0) {
#pragma unroll
        for (int i = 0; i < 7; ++i) xb[i] = xq[28 + 2 * i];
      }
      if (t == 7) {
#pragma unroll
        for (int i = 7; i < 14; ++i) xb[i] = xq[28 + 2 * i];
      }
      __builtin_amdgcn_sched_barrier(0);
    }
    f32x16 pq;
#pragma unroll
    for (int r = 0; r < 16; ++r) pq[r] = 0.f;
    pq = MFMA32(tr_frag(wpb, wpb + 512), pack_acc(accQ, 0), pq);
    pq = MFMA32(tr_frag(wpb + 1024, wpb + 1536), pack_acc(accQ, 1), pq);
    dual_softmax(pq, hh, phq_pos, phq_neg);
  } else {
    bar_lds();
    acc_add(cslot, accK, lane); acc_add(cslot + 4096, accV, lane);
    f32x16 pk;
#pragma unroll
    for (int r = 0; r < 16; ++r) pk[r] = 0.f;
    pk = MFMA32(tr_frag(wpb, wpb + 512), pack_acc(accK, 0), pk);
    pk = MFMA32(tr_frag(wpb + 1024, wpb + 1536), pack_acc(accK, 1), pk);
    u32x4 kpos[2], kneg[2];
    dual_softmax(pk, hh, kpos, kneg);
    char* phw = sm + B_PHI + wr * B_PHIW;
#pragma unroll
    for (int gq = 0; gq < 4; ++gq) {
      *(u32x2*)(phw + c32 * 136 + (8 * gq + 4 * hh) * 2) = u32x2{kpos[gq >> 1][2 * (gq & 1)], kpos[gq >> 1][2 * (gq & 1) + 1]};
      *(u32x2*)(phw + c32 * 136 + 64 + (8 * gq + 4 * hh) * 2) = u32x2{kneg[gq >> 1][2 * (gq & 1)], kneg[gq >> 1][2 * (gq & 1) + 1]};
    }
    {
      const int cone = off ? 0 : 28;
      const bool valid = (c32 >= off) && (c32 < off + 28);
#pragma unroll
      for (int r = 0; r < 16; ++r) accV[r] = valid ? accV[r] : ((c32 == cone) ? 1.f : 0.f);
    }
    const bf16x8 vp0 = pack_acc(accV, 0), vp1 = pack_acc(accV, 1);
    f32x16 kvp[2];
#pragma unroll
    for (int m = 0; m < 2; ++m) {
#pragma unroll
      for (int r = 0; r < 16; ++r) kvp[m][r] = 0.f;
      const char* pb = phw + (4 * hh + q4) * 136 + (32 * m + 16 * cb + 4 * p4) * 2;
      const bf16x8 a0 = tr_frag(pb, pb + 8 * 136);
      const bf16x8 a1 = tr_frag(pb + 16 * 136, pb + 24 * 136);
      kvp[m] = MFMA32(a0, vp0, kvp[m]);
      kvp[m] = MFMA32(a1, vp1, kvp[m]);
    }
    acc_store(cslot, kvp[0], lane); acc_store(cslot + 4096, kvp[1], lane);
    bar_lds();
    {
      const unsigned base = WS_KVP + (unsigned)(h * 16 + rt) * 8192u;
#pragma unroll
      for (int i = 0; i < 2; ++i) {
        const int ck = 2 * wr + i;
        const char* q = sm + B_COMB + (ck * 64 + lane) * 16;
        const f32x4 v = (*(const f32x4*)(q) + *(const f32x4*)(q + 8192)) + (*(const f32x4*)(q + 16384) + *(const f32x4*)(q + 24576));
        st16_wt(wsr, base + (ck * 64 + lane) * 16, __builtin_bit_cast(u32x4, v));
      }
    }
    wait_vm0();
    if (lane == 0) {
      if (atomicAdd((unsigned*)(sm + B_CNT), 1u) == 3u) st_flag(P.ws, WS_FKV + 128 * h + 8 * rt, tag);
    }
#pragma unroll
    for (int s = 0; s < 2; ++s)
#pragma unroll
      for (int j = 0; j < 4; ++j) { phq_pos[s][j] = 0u; phq_neg[s][j] = 0u; }
  }
  STAMP(3);
  bar_lds();
  STAMP(4);
  const int rt2 = 4 * xcd + (jj & 3), ct = jj >> 2;
  for (int i = w; i < 49; i += 8) {
    const int id = i * 64 + lane;
    const int row = id / 7, c16 = id - 7 * row;
    glds16(P.wo + (size_t)row * D_ + 56 * ct + 8 * c16, sm + B_W + i * 1024);
  }
  if (tid < 16) *(unsigned*)(sm + B_W + 50176 + tid * 4) = 0u;
  if (w == 0) wait_flags(P.ws, WS_FKV + 128 * h, 16, tag);
  bar_lds();
  STAMP(5);
  {
    f32x4 s = {0.f, 0.f, 0.f, 0.f};
    u32x4 v[16];
#pragma unroll
    for (int r = 0; r < 16; ++r) v[r] = ld16_wt(wsr, WS_KVP + (unsigned)(h * 16 + r) * 8192u + tid * 16);
#pragma unroll
    for (int r = 0; r < 16; ++r) s += __builtin_bit_cast(f32x4, v[r]);
    const int m = tid >> 8, gq = (tid >> 6) & 3;
    const int f = 32 * m + 8 * gq + 4 * hh;
    unsigned short* kl = (unsigned short*)(sm + B_KVL);
    const unsigned p0 = pack_bf16(s[0], s[1]), p1 = pack_bf16(s[2], s[3]);
    kl[(f + 0) * 32 + c32] = (unsigned short)(p0 & 0xffffu);
    kl[(f + 1) * 32 + c32] = (unsigned short)(p0 >> 16);
    kl[(f + 2) * 32 + c32] = (unsigned short)(p1 & 0xffffu);
    kl[(f + 3) * 32 + c32] = (unsigned short)(p1 >> 16);
  }
  bar_lds();
  STAMP(6);
  if (kh == 1) {
    f32x16 num;
#pragma unroll
    for (int r = 0; r < 16; ++r) num[r] = 0.f;
#pragma unroll
    for (int m = 0; m < 2; ++m)
#pragma unroll
      for (int s = 0; s < 2; ++s) {
        const char* pb = sm + B_KVL + (32 * m + 16 * s + 4 * hh + q4) * 64 + (16 * cb + 4 * p4) * 2;
        const bf16x8 a = tr_frag(pb, pb + 8 * 64);
        num = MFMA32(a, as_frag(m == 0 ? phq_pos[s] : phq_neg[s]), num);
      }
    const float dsrc = off ? num[0] : num[12];
    float den = __shfl(dsrc, c32 + (off ? 0 : 32));
    den = bfr(bfr(den) + 1e-6f);
    const float rden = 1.f / den;
    u32x2 ov[4];
#pragma unroll
    for (int gq = 0; gq < 4; ++gq) {
      const int d = 8 * gq + 4 * hh - off;
      const bool ok = (d >= 0) && (d < 28);
      ov[gq][0] = ok ? pack_bf16(bfr(num[4 * gq]) / den, bfr(num[4 * gq + 1]) / den) : 0u;
      ov[gq][1] = ok ? pack_bf16(bfr(num[4 * gq + 2]) / den, bfr(num[4 * gq + 3]) / den) : 0u;
    }
    (void)rden;
    const unsigned ob = WS_O + (unsigned)((4 * rt + wr) * 32 + 2 * h) * 1024u + lane * 16;
#pragma unroll
    for (int pr = 0; pr < 2; ++pr) {
      const auto r0 = __builtin_amdgcn_permlane32_swap(ov[2 * pr][0], ov[2 * pr + 1][0], false, false);
      const auto r1 = __builtin_amdgcn_permlane32_swap(ov[2 * pr][1], ov[2 * pr + 1][1], false, false);
      st16_wt(wsr, ob + pr * 1024, u32x4{r0[0], r1[0], r0[1], r1[1]});
    }
  }
  wait_vm0();
  bar_lds();
  if (tid == 0) st_flag(P.ws, WS_FO + 128 * rt + 8 * h, tag);
  STAMP(7);
  if (w == 0) wait_flags(P.ws, WS_FO + 128 * (rt2 >> 1), 16, tag);
  bar_lds();
  STAMP(8);
  {
    const int wr2 = w & 1, wc = (w >> 1) & 1;
    bf16x8 of[16];
    const unsigned ob = WS_O + (unsigned)((2 * rt2 + wr2) * 32 + 16 * kh) * 1024u + lane * 16;
#pragma unroll
    for (int t = 0; t < 16; ++t) of[t] = as_frag(ld16_wt(wsr, ob + 1024 * t));
    f32x16 acc;
#pragma unroll
    for (int r = 0; r < 16; ++r) acc[r] = 0.f;
    const char* wb = sm + B_W + (224 * kh + 8 * hh + q4) * 112 + (28 * wc + 16 * cb + 4 * p4) * 2;
#define WROW(t) ((((28 * ((t) >> 1)) & ~7) + 16 * ((t) & 1)) * 112)
    bf16x8 wf[3];
    wf[0] = tr_frag(wb + WROW(0), wb + WROW(0) + 448); wf[1] = tr_frag(wb + WROW(1), wb + WROW(1) + 448);
#pragma unroll
    for (int t = 0; t < 16; ++t) {
      if (t + 2 < 16) wf[(t + 2) % 3] = tr_frag(wb + WROW(t + 2), wb + WROW(t + 2) + 448);
      acc = MFMA32(wf[t % 3], of[t], acc);
      __builtin_amdgcn_sched_barrier(0);
    }
    STAMP(11);
    char* c2 = sm + B_CMB2 + (w & 3) * 4096;
    if (kh == 1) acc_store(c2, acc, lane);
    bar_lds();
    if (kh == 0) {
      acc_add(c2, acc, lane);
      unsigned short* dp = P.out + (size_t)(64 * rt2 + 32 * wr2 + c32) * OUTW + 448 + 56 * ct + 28 * wc;
#pragma unroll
      for (int gq = 0; gq < 4; ++gq) {
        const int c = 8 * gq + 4 * hh;
        if (c < 28) {
          u32x2 v;
          v[0] = pack_bf16(acc[4 * gq], acc[4 * gq + 1]);
          v[1] = pack_bf16(acc[4 * gq + 2], acc[4 * gq + 3]);
          *(u32x2*)(dp + c) = v;
        }
      }
    }
  }
  if (b == 0 && tid == 0) st_flag(P.ws, WS_EB, tag);
  STAMP(9);
}

__global__ void __launch_bounds__(512, 4) fused_kernel(Params p) {
  extern __shared__ __attribute__((aligned(16))) char sm[];
  const int b = blockIdx.x;
  if (b < 256) part_b(sm, p, b);
  else part_a(sm, p, b - 256);
}

extern "C" void kernel_launch(void* const* d_in, const int* in_sizes, int n_in,
                              void* d_out, int out_size, void* d_ws, size_t ws_size,
                              hipStream_t stream) {
  Params p;
  p.u = (const unsigned short*)d_in[0];
  p.logA = (const unsigned short*)d_in[1];
  p.Aim = (const unsigned short*)d_in[2];
  p.Cre = (const unsigned short*)d_in[3];
  p.Cim = (const unsigned short*)d_in[4];
  p.logdt = (const unsigned short*)d_in[5];
  p.Dsk = (const unsigned short*)d_in[6];
  p.x = (const unsigned short*)d_in[7];
  p.wqkv = (const unsigned short*)d_in[8];
  p.wphi = (const unsigned short*)d_in[9];
  p.wo = (const unsigned short*)d_in[10];
  p.out = (unsigned short*)d_out;
  p.ws = (char*)d_ws;
  (void)hipFuncSetAttribute((const void*)fused_kernel, hipFuncAttributeMaxDynamicSharedMemorySize, LDS_BYTES);
  fused_kernel<<<480, 512, LDS_BYTES, stream>>>(p);
}
```

```cpp
#include <hip/hip_runtime.h>
#include <hip/hip_bf16.h>
#include <stdint.h>

using bf16x8 = __attribute__((ext_vector_type(8))) short;
using s16x4  = __attribute__((ext_vector_type(4))) short;
using f32x16 = __attribute__((ext_vector_type(16))) float;
using f32x4  = __attribute__((ext_vector_type(4))) float;
using u32x4  = __attribute__((ext_vector_type(4))) unsigned;
using u32x2  = __attribute__((ext_vector_type(2))) unsigned;
typedef __bf16 bfx2_t __attribute__((ext_vector_type(2)));
typedef float  f32x2  __attribute__((ext_vector_type(2)));
typedef unsigned long long u64;

#define DI __device__ __forceinline__
#define LAS __attribute__((address_space(3)))
#define GAS __attribute__((address_space(1)))
#define MFMA32(a, b, c) __builtin_amdgcn_mfma_f32_32x32x16_bf16((a), (b), (c), 0, 0, 0)
#define MFMAF(a, b, c) __builtin_amdgcn_mfma_f32_16x16x4f32((a), (b), (c), 0, 0, 0)
#define MFMA16(a, b, c) __builtin_amdgcn_mfma_f32_16x16x32_bf16((a), (b), (c), 0, 0, 0)

constexpr int D_  = 448;
constexpr int N_  = 256;
constexpr int OUTW = 896;

#define STAMP(i)
#define WSTAMP(i)

constexpr unsigned WS_EB  = 0;
constexpr unsigned WS_EA  = 256;
constexpr unsigned WS_FKV = 4096;
constexpr unsigned WS_FO  = 8192;
constexpr unsigned WS_FUT = 12288;
constexpr unsigned WS_KVP = 1u << 20;
constexpr unsigned WS_O   = 4u << 20;
constexpr unsigned WS_UT  = 8u << 20;
constexpr unsigned WS_END = 10u << 20;

DI unsigned pack_bf16(float lo, float hi) {
  f32x2 v = {lo, hi};
  return __builtin_bit_cast(unsigned, __builtin_convertvector(v, bfx2_t));
}
DI float bf_lo(unsigned p) { return __uint_as_float(p << 16); }
DI float bf_hi(unsigned p) { return __uint_as_float(p & 0xffff0000u); }
DI float bf2f(unsigned short b) { return __uint_as_float(((unsigned)b) << 16); }
DI float bfr(float x) { return bf_lo(pack_bf16(x, 0.f)); }
DI s16x4 tr_read(const char* p) {
  return __builtin_amdgcn_ds_read_tr16_b64_v4i16((LAS s16x4*)(p));
}
DI bf16x8 tr_frag(const char* p0, const char* p1) {
  s16x4 a = tr_read(p0), b = tr_read(p1);
  return __builtin_shufflevector(a, b, 0, 1, 2, 3, 4, 5, 6, 7);
}
DI bf16x8 as_frag(u32x4 v) { return __builtin_bit_cast(bf16x8, v); }
DI void glds16(const void* g, void* lds_wave_base) {
  __builtin_amdgcn_global_load_lds((const unsigned*)g, (unsigned*)lds_wave_base, 16, 0, 0);
}
DI void wait_vm0() { asm volatile("s_waitcnt vmcnt(0)" ::: "memory"); }
DI void bar_lds() { asm volatile("s_waitcnt lgkmcnt(0)" ::: "memory"); __builtin_amdgcn_s_barrier(); asm volatile("" ::: "memory"); }

typedef __amdgpu_buffer_rsrc_t rsrc_t;
DI void st16_wt(rsrc_t r, unsigned off, u32x4 v) { __builtin_amdgcn_raw_buffer_store_b128(v, r, off, 0, 16); }
DI void st8_wt(rsrc_t r, unsigned off, u32x2 v) { __builtin_amdgcn_raw_buffer_store_b64(v, r, off, 0, 16); }
DI u32x4 ld16_wt(rsrc_t r, unsigned off) { return __builtin_amdgcn_raw_buffer_load_b128(r, off, 0, 16); }
DI unsigned ld4_wt(rsrc_t r, unsigned off) { return __builtin_amdgcn_raw_buffer_load_b32(r, off, 0, 16); }
DI u64 ld_flag(const char* ws, unsigned off) {
  return __hip_atomic_load((GAS u64*)(ws + off), __ATOMIC_RELAXED, __HIP_MEMORY_SCOPE_AGENT);
}
DI void st_flag(char* ws, unsigned off, u64 v) {
  __hip_atomic_store((GAS u64*)(ws + off), v, __ATOMIC_RELAXED, __HIP_MEMORY_SCOPE_AGENT);
}
DI void wait_flags(const char* ws, unsigned off, int n, u64 tag) {
  const int lane = threadIdx.x & 63;
  for (int it = 0; it < (1 << 16); ++it) {
    const u64 v = (lane < n) ? ld_flag(ws, off + 8 * lane) : tag;
    if (__all(v == tag)) break;
    __builtin_amdgcn_s_sleep(1);
  }
  asm volatile("" ::: "memory");
}
DI u64 uniform64(u64 v) {
  const unsigned lo = __builtin_amdgcn_readfirstlane((unsigned)v), hi = __builtin_amdgcn_readfirstlane((unsigned)(v >> 32));
  return ((u64)hi << 32) | lo;
}

struct Params {
  const unsigned short *u, *logA, *Aim, *Cre, *Cim, *logdt, *Dsk, *x, *wqkv, *wphi, *wo;
  unsigned short* out;
  char* ws;
};

constexpr int LDS_BYTES = 80 * 1024;

constexpr int A_UCOPY = 4160;
constexpr int A_US    = 0;
constexpr int A_CH0   = 4 * A_UCOPY;
constexpr int A_KRH   = 0;
constexpr int A_KRL   = 4576;
constexpr int A_KD    = 9152;
constexpr int A_PR2   = A_KD + 4112;
constexpr int A_RED   = A_PR2 + 2048;
constexpr int A_SUM   = A_RED + 16384;
constexpr int A_CHS   = A_SUM + 64;
constexpr int A_CNT   = A_CH0 + 2 * A_CHS;
static_assert(A_CNT + 16 <= LDS_BYTES, "A lds");

DI void cexp_eval(float lr, float li, float t, float& er, float& ei) {
  const float xr = lr * t;
  const float e  = __builtin_amdgcn_exp2f(xr * 1.44269504088896340736f);
  const float x  = li * t;
  const float C_HI = 0.15915494309189533577f;
  const float C_LO = (float)(0.15915494309189533577 - (double)0.15915494309189533577f);
  const float rh = x * C_HI;
  float rl = __builtin_fmaf(x, C_HI, -rh);
  rl = __builtin_fmaf(x, C_LO, rl);
  const float rev = (rh - __builtin_rintf(rh)) + rl;
  er = e * __builtin_amdgcn_cosf(rev);
  ei = e * __builtin_amdgcn_sinf(rev);
}
DI void cexp_fast(const f32x4& p, float t, float& er, float& ei) {
  const float e = __builtin_amdgcn_exp2f(p[0] * t);
  const float rev = __builtin_fmaf(t, p[2], __builtin_amdgcn_fractf(t * p[1]));
  er = e * __builtin_amdgcn_cosf(rev);
  ei = e * __builtin_amdgcn_sinf(rev);
}
DI void split2(float a, float b, unsigned& hi, unsigned& lo) {
  hi = pack_bf16(a, b);
  lo = pack_bf16(a - bf_lo(hi), b - bf_hi(hi));
}
DI void split2v(const f32x2& R, const f32x2& I, u32x4& h, u32x4& l, int q, unsigned mlo, unsigned mhi) {
  const unsigned h0 = pack_bf16(R[0], I[0]), h1 = pack_bf16(R[1], I[1]);
  h[2 * q] = h0; h[2 * q + 1] = h1;
  float r0, i0, r1, i1;
  asm("v_dot2_f32_bf16 %0, %4, %6, %8\n\tv_dot2_f32_bf16 %1, %4, %7, %9\n\t"
      "v_dot2_f32_bf16 %2, %5, %6, %10\n\tv_dot2_f32_bf16 %3, %5, %7, %11\n\ts_nop 2"
      : "=&v"(r0), "=&v"(i0), "=&v"(r1), "=&v"(i1)
      : "v"(h0), "v"(h1), "s"(mlo), "s"(mhi), "v"(R[0]), "v"(I[0]), "v"(R[1]), "v"(I[1]));
  l[2 * q] = pack_bf16(r0, i0);
  l[2 * q + 1] = pack_bf16(r1, i1);
}
DI void bar4(char* cnt, unsigned& target) {
  asm volatile("s_waitcnt lgkmcnt(0)" ::: "memory");
  target += 4u;
  if ((threadIdx.x & 63) == 0) __hip_atomic_fetch_add((LAS unsigned*)cnt, 1u, __ATOMIC_RELAXED, __HIP_MEMORY_SCOPE_WORKGROUP);
  while ((unsigned)__builtin_amdgcn_readfirstlane(__hip_atomic_load((LAS unsigned*)cnt, __ATOMIC_RELAXED, __HIP_MEMORY_SCOPE_WORKGROUP)) < target)
    __builtin_amdgcn_s_sleep(1);
  asm volatile("" ::: "memory");
}

constexpr int bf16_round_int(int t) {
  if (t < 256) return t;
  int sh = 0;
  while ((t >> sh) >= 256) ++sh;
  const int q = t >> sh, r = t & ((1 << sh) - 1), half = 1 << (sh - 1);
  return (q + ((r > half || (r == half && (q & 1))) ? 1 : 0)) << sh;
}
constexpr int kd_slot(int tap) {
  if (tap < 0) return 1025;
  const int p = bf16_round_int(tap);
  if (p < 256) return p;
  if (p >= 2048) return 1024;
  int s = 0;
  while ((p >> s) >= 256) ++s;
  return s * 256 + (p >> s);
}
struct KTab { unsigned short v[2304]; };
constexpr KTab make_ktab() {
  KTab t{};
  for (int i = 0; i < 2304; ++i) t.v[i] = (unsigned short)(4 * kd_slot(2047 - i));
  return t;
}
__device__ __attribute__((aligned(16))) const KTab KTAB = make_ktab();

DI void part_a(char* sm, const Params& P, int a) {
  const int tid = threadIdx.x, lane = tid & 63, w = __builtin_amdgcn_readfirstlane(tid >> 6);
  const int ch = w >> 2, cw = w & 3, ctid = tid & 255;
  const int i16 = lane & 15, g = lane >> 4;
  const int d0 = 56 * (a & 7) + 2 * (a >> 3);
  const int cg = d0 >> 6, r32 = (d0 & 63) >> 1;
  const int d = d0 + ch;
  char* cs = sm + A_CH0 + ch * A_CHS;
  const rsrc_t wsr = __builtin_amdgcn_make_buffer_rsrc(P.ws, 0, WS_END, 0x00020000);
  STAMP(0);
  const u64 ep = ld_flag(P.ws, WS_EA + 256 * cg);
  const unsigned short pla = P.logA[d * N_ + ctid], pai = P.Aim[d * N_ + ctid];
  const unsigned short pcr = P.Cre[d * N_ + ctid], pci = P.Cim[d * N_ + ctid];
  const unsigned short pdt = P.logdt[d];
  const u32x4 ut = *(const u32x4*)(P.u + (size_t)(64 * r32 + (tid >> 3)) * D_ + 64 * cg + 8 * (tid & 7));
  {
    const float dt = bfr(expf(bf2f(pdt)));
    const float am = bfr(expf(bf2f(pla)));
    const float lr = -am * dt, li = bf2f(pai) * dt;
    const float cr = bf2f(pcr) * dt, ci = bf2f(pci) * dt;
    {
      const float C_HI = 0.15915494309189533577f;
      const float C_LO = (float)(0.15915494309189533577 - (double)0.15915494309189533577f);
      const float q = li * C_HI;
      const float er = __builtin_fmaf(li, C_LO, __builtin_fmaf(li, C_HI, -q));
      const float a16 = __uint_as_float(__float_as_uint(q) & 0xffffff00u);
      *(f32x4*)(cs + A_KD + ctid * 16) = f32x4{lr * 1.44269504088896340736f, a16, (q - a16) + er, 0.f};
      *(f32x2*)(cs + A_PR2 + ctid * 8) = f32x2{cr, ci};
    }
    float er, ei;
    cexp_eval(lr, li, 2048.f, er, ei);
    float t = cr * er - ci * ei;
#pragma unroll
    for (int o = 32; o >= 1; o >>= 1) t += __shfl_xor(t, o);
    if (lane == 0) ((float*)(cs + A_SUM))[cw] = t;
  }
  const u64 tag = uniform64(ep) + 1ull;
  {
    unsigned short* T = (unsigned short*)(sm + A_US);
    const int row = tid >> 3, c8 = 8 * (tid & 7);
#pragma unroll
    for (int j = 0; j < 4; ++j) {
      T[(c8 + 2 * j) * 72 + row] = (unsigned short)(ut[j] & 0xffffu);
      T[(c8 + 2 * j + 1) * 72 + row] = (unsigned short)(ut[j] >> 16);
    }
    if (tid < 3) *(unsigned*)(sm + A_CNT + 4 * tid) = 0u;
    bar_lds();
    if (w < 4) {
#pragma unroll
      for (int i = 0; i < 2; ++i) {
        const int c = (tid >> 3) + 32 * i;
        const u32x4 v = *(const u32x4*)(T + c * 72 + 8 * (tid & 7));
        st16_wt(wsr, WS_UT + ((64 * cg + c) * 2048 + 64 * r32 + 8 * (tid & 7)) * 2, v);
      }
    }
  }
  STAMP(1);
  u32x4 uv, kt0, kt1; unsigned unx = 0u;
  char* cb4 = sm + A_CNT + 4 + 4 * ch;
  unsigned b4t = 0u;
  {
    f32x4 acc[4];
#pragma unroll
    for (int s = 0; s < 4; ++s) acc[s] = f32x4{0.f, 0.f, 0.f, 0.f};
    const float ty = (float)i16, tx = (float)(16 * i16);
    unsigned mlo = 0x0000bf80u, mhi = 0xbf800000u;
    asm volatile("" : "+s"(mlo), "+s"(mhi));
    WSTAMP(0);
#pragma unroll 1
    for (int ks = 0; ks < 4; ++ks) {
      if (ks == 3) WSTAMP(1);
      if (ks == 1 && w < 4) {
        wait_vm0();
        if (lane == 0 && atomicAdd((unsigned*)(sm + A_CNT), 1u) == 3u) st_flag(P.ws, WS_FUT + 256 * cg + 8 * r32, tag);
      }
      if (ks == 3) {
        wait_flags(P.ws, WS_FUT + 256 * cg, 32, tag);
        uv = ld16_wt(wsr, WS_UT + (d * 2048 + 8 * ctid) * 2);
        if (ctid < 255) unx = ld4_wt(wsr, WS_UT + (d * 2048 + 8 * ctid + 8) * 2);
        kt0 = *(const u32x4*)(KTAB.v + 8 * ctid);
        kt1 = *(const u32x4*)(KTAB.v + 2048 + 8 * (ctid & 31));
      }
      f32x2 YR[2], YI[2], XR[2], XI[2], CR[2], CI[2];
      const char* pp = cs + A_KD + (64 * cw + 16 * ks + 4 * g) * 16;
      const char* pc = cs + A_PR2 + (64 * cw + 16 * ks + 4 * g) * 8;
#pragma unroll
      for (int m = 0; m < 4; ++m) {
        const f32x4 p = *(const f32x4*)(pp + m * 16);
        float r0, i0, r1, i1;
        cexp_fast(p, ty, r0, i0);
        cexp_fast(p, tx, r1, i1);
        YR[m >> 1][m & 1] = r0; YI[m >> 1][m & 1] = i0;
        XR[m >> 1][m & 1] = r1; XI[m >> 1][m & 1] = i1;
      }
#pragma unroll
      for (int q = 0; q < 2; ++q) {
        const f32x4 c = *(const f32x4*)(pc + q * 16);
        CR[q] = f32x2{c[0], c[2]}; CI[q] = f32x2{c[1], c[3]};
      }
#pragma unroll
      for (int s = 0; s < 4; ++s) {
        u32x4 ah, al, bh, bl;
#pragma unroll
        for (int q = 0; q < 2; ++q) {
          const f32x2 zr = CR[q] * XR[q] - CI[q] * XI[q];
          const f32x2 zn = -(CR[q] * XI[q]) - CI[q] * XR[q];
          split2v(YR[q], YI[q], ah, al, q, mlo, mhi);
          split2v(zr, zn, bh, bl, q, mlo, mhi);
        }
        acc[s] = MFMA16(as_frag(ah), as_frag(bh), acc[s]);
        acc[s] = MFMA16(as_frag(ah), as_frag(bl), acc[s]);
        acc[s] = MFMA16(as_frag(al), as_frag(bh), acc[s]);
        if (s < 3) {
#pragma unroll
          for (int q = 0; q < 2; ++q) {
            const f32x2 a = YR[q], b = YI[q], c = XR[q], e = XI[q];
            YR[q] = a * a - b * b; YI[q] = (a + a) * b;
            XR[q] = c * c - e * e; XI[q] = (c + c) * e;
          }
        }
      }
    }
    WSTAMP(2);
#pragma unroll
    for (int s = 0; s < 4; ++s)
      *(f32x4*)(cs + A_RED + ((cw * 4 + s) * 256 + 16 * i16 + 4 * g) * 4) = acc[s];
  }
  bar4(cb4, b4t);
  STAMP(2);
  {
    const float* red = (const float*)(cs + A_RED);
    unsigned* kp = (unsigned*)(cs + A_KD);
#pragma unroll
    for (int s = 0; s < 4; ++s) {
      const float v = 2.f * ((red[(0 * 4 + s) * 256 + ctid] + red[(1 * 4 + s) * 256 + ctid]) +
                             (red[(2 * 4 + s) * 256 + ctid] + red[(3 * 4 + s) * 256 + ctid]));
      const float vh = bfr(v);
      kp[s * 256 + ctid] = pack_bf16(vh, v - vh);
    }
    if (ctid == 0) {
      const float* sp = (const float*)(cs + A_SUM);
      const float v = 2.f * ((sp[0] + sp[1]) + (sp[2] + sp[3]));
      const float vh = bfr(v);
      kp[1024] = pack_bf16(vh, v - vh);
      kp[1025] = 0u;
    }
  }
  bar4(cb4, b4t);
  {
    const char* kp = cs + A_KD;
#pragma unroll
    for (int part = 0; part < 2; ++part) {
      if (part == 0 || ctid < 30) {
        const u32x4 kt = part ? kt1 : kt0;
        u32x4 h, l;
#pragma unroll
        for (int j = 0; j < 4; ++j) {
          const unsigned a = *(const unsigned*)(kp + (kt[j] & 0xffffu));
          const unsigned b = *(const unsigned*)(kp + (kt[j] >> 16));
          h[j] = __builtin_amdgcn_perm(b, a, 0x05040100u);
          l[j] = __builtin_amdgcn_perm(b, a, 0x07060302u);
        }
        const int c = ctid + 256 * part;
        *(u32x4*)(cs + A_KRH + c * 16) = h;
        *(u32x4*)(cs + A_KRL + c * 16) = l;
      }
    }
  }
  {
    char* c0p = sm + A_US + (2 * ch) * A_UCOPY;
    char* c1p = c0p + A_UCOPY;
    *(u32x4*)(c0p + (8 * ctid + 16) * 2) = uv;
    u32x4 sh;
    sh[0] = (uv[0] >> 16) | (uv[1] << 16); sh[1] = (uv[1] >> 16) | (uv[2] << 16);
    sh[2] = (uv[2] >> 16) | (uv[3] << 16); sh[3] = (uv[3] >> 16) | (unx << 16);
    *(u32x4*)(c1p + (8 * ctid + 16) * 2) = sh;
    if (ctid == 0) {
      *(u32x4*)(c0p) = u32x4{0u, 0u, 0u, 0u}; *(u32x4*)(c0p + 16) = u32x4{0u, 0u, 0u, 0u};
      *(u32x4*)(c1p) = u32x4{0u, 0u, 0u, 0u}; *(u32x4*)(c1p + 16) = u32x4{0u, 0u, 0u, uv[0] << 16};
    }
  }
  bar4(cb4, b4t);
  STAMP(3);
  f32x4 acc[8];
  {
#pragma unroll
    for (int q = 0; q < 8; ++q) acc[q] = f32x4{0.f, 0.f, 0.f, 0.f};
    const int par = (i16 + 1) & 1;
    const char* ubase = sm + A_US + (2 * ch + par) * A_UCOPY + 2 * (i16 + 1 - par + 8 * g);
#pragma unroll 1
    for (int cc = 0; cc < 2; ++cc) {
      const int r = 2 * cw + cc;
      bf16x8 af[8];
#pragma unroll
      for (int q = 0; q < 8; ++q) {
        const unsigned* p = (const unsigned*)(ubase + 64 * (r + 8 * q));
        u32x4 v; v[0] = p[0]; v[1] = p[1]; v[2] = p[2]; v[3] = p[3];
        af[q] = as_frag(v);
      }
      const int kb = 2 * (2032 - 16 * i16 + 32 * r + 8 * g);
#pragma unroll
      for (int b = 0; b < 8; ++b) {
        const bf16x8 bh = *(const bf16x8*)(cs + A_KRH + kb - 512 * b);
        const bf16x8 bl = *(const bf16x8*)(cs + A_KRL + kb - 512 * b);
#pragma unroll
        for (int q = 0; q + b < 8; ++q) acc[q + b] = MFMA16(af[q], bh, acc[q + b]);
#pragma unroll
        for (int q = 0; q + b < 8; ++q) acc[q + b] = MFMA16(af[q], bl, acc[q + b]);
      }
    }
  }
  STAMP(4);
  const unsigned short pds = P.Dsk[d];
  f32x4 y2[2];
  {
    const bool up = (cw & 2) != 0;
    f32x4 keep[4];
#pragma unroll
    for (int i = 0; i < 4; ++i) {
      keep[i] = up ? acc[4 + i] : acc[i];
      const f32x4 snd = up ? acc[i] : acc[4 + i];
      *(f32x4*)(cs + A_RED + ((cw * 4 + i) * 64 + lane) * 16) = snd;
    }
    bar4(cb4, b4t);
#pragma unroll
    for (int i = 0; i < 4; ++i) keep[i] += *(const f32x4*)(cs + A_RED + (((cw ^ 2) * 4 + i) * 64 + lane) * 16);
    const bool od = (cw & 1) != 0;
#pragma unroll
    for (int i = 0; i < 2; ++i) {
      y2[i] = od ? keep[2 + i] : keep[i];
      const f32x4 snd = od ? keep[i] : keep[2 + i];
      *(f32x4*)(cs + A_KRH + ((cw * 2 + i) * 64 + lane) * 16) = snd;
    }
    bar4(cb4, b4t);
#pragma unroll
    for (int i = 0; i < 2; ++i) y2[i] += *(const f32x4*)(cs + A_KRH + (((cw ^ 1) * 2 + i) * 64 + lane) * 16);
  }
  {
    const float ds = bf2f(pds);
    const unsigned short* uc = (const unsigned short*)(sm + A_US + (2 * ch) * A_UCOPY) + 16;
#pragma unroll
    for (int i = 0; i < 2; ++i) {
      const int l = 256 * (4 * (cw >> 1) + 2 * (cw & 1) + i) + 16 * i16 + 4 * g;
      const u32x2 uu = *(const u32x2*)(uc + l);
      u32x2 o;
      o[0] = pack_bf16(bfr(y2[i][0]) + bfr(bf_lo(uu[0]) * ds), bfr(y2[i][1]) + bfr(bf_hi(uu[0]) * ds));
      o[1] = pack_bf16(bfr(y2[i][2]) + bfr(bf_lo(uu[1]) * ds), bfr(y2[i][3]) + bfr(bf_hi(uu[1]) * ds));
      *(u32x2*)(cs + A_KD + l * 2) = o;
    }
  }
  bar_lds();
  {
    const unsigned short* o0 = (const unsigned short*)(sm + A_CH0 + A_KD);
    const unsigned short* o1 = (const unsigned short*)(sm + A_CH0 + A_CHS + A_KD);
#pragma unroll
    for (int i = 0; i < 4; ++i) {
      const int l = tid + 512 * i;
      *(unsigned*)(P.out + (size_t)l * OUTW + d0) = (unsigned)o0[l] | ((unsigned)o1[l] << 16);
    }
  }
  if (r32 == 0 && tid == 0) st_flag(P.ws, WS_EA + 256 * cg, tag);
  STAMP(5);
}

DI bf16x8 pack_acc(const f32x16& x, int s) {
  u32x4 p;
#pragma unroll
  for (int j = 0; j < 4; ++j) p[j] = pack_bf16(x[8 * s + 2 * j], x[8 * s + 2 * j + 1]);
  return as_frag(p);
}

DI void dual_softmax(const f32x16& pacc, int hh, u32x4 (&pos)[2], u32x4 (&neg)[2]) {
  float p[16];
#pragma unroll
  for (int r = 0; r < 16; ++r) p[r] = bfr(pacc[r]);
  float mx = p[0], mn = p[0];
#pragma unroll
  for (int r = 1; r < 12; ++r) { mx = fmaxf(mx, p[r]); mn = fminf(mn, p[r]); }
#pragma unroll
  for (int r = 12; r < 16; ++r) { mx = hh ? mx : fmaxf(mx, p[r]); mn = hh ? mn : fminf(mn, p[r]); }
  mx = fmaxf(mx, __shfl_xor(mx, 32));
  mn = fminf(mn, __shfl_xor(mn, 32));
  float ep[16], en[16], sp = 0.f, sn = 0.f;
#pragma unroll
  for (int r = 0; r < 16; ++r) {
    const bool valid = (r < 12) || (hh == 0);
    ep[r] = valid ? __expf(p[r] - mx) : 0.f;
    en[r] = valid ? __expf(mn - p[r]) : 0.f;
    sp += ep[r]; sn += en[r];
  }
  sp += __shfl_xor(sp, 32);
  sn += __shfl_xor(sn, 32);
  const float rp = __builtin_amdgcn_rcpf(sp), rn = __builtin_amdgcn_rcpf(sn);
#pragma unroll
  for (int s = 0; s < 2; ++s)
#pragma unroll
    for (int j = 0; j < 4; ++j) {
      pos[s][j] = pack_bf16(ep[8 * s + 2 * j] * rp, ep[8 * s + 2 * j + 1] * rp);
      neg[s][j] = pack_bf16(en[8 * s + 2 * j] * rn, en[8 * s + 2 * j + 1] * rn);
    }
}
DI void acc_store(char* p, const f32x16& a, int lane) {
#pragma unroll
  for (int gq = 0; gq < 4; ++gq)
    *(f32x4*)(p + (gq * 64 + lane) * 16) = f32x4{a[4 * gq], a[4 * gq + 1], a[4 * gq + 2], a[4 * gq + 3]};
}
DI void acc_add(const char* p, f32x16& a, int lane) {
#pragma unroll
  for (int gq = 0; gq < 4; ++gq) {
    const f32x4 v = *(const f32x4*)(p + (gq * 64 + lane) * 16);
    a[4 * gq] += v[0]; a[4 * gq + 1] += v[1]; a[4 * gq + 2] += v[2]; a[4 * gq + 3] += v[3];
  }
}

constexpr int B_W    = 0;
constexpr int B_COMB = 28672;
constexpr int B_PHI  = 61440;
constexpr int B_PHIW = 32 * 136;
constexpr int B_KVL  = 61440;
constexpr int B_WPH  = 79872;
constexpr int B_XS   = 57344;
constexpr int B_CNT  = 79360;
constexpr int B_CMB2 = 53248;

DI void part_b(char* sm, const Params& P, int b) {
  const int tid = threadIdx.x, lane = tid & 63, w = __builtin_amdgcn_readfirstlane(tid >> 6);
  const int wr = w & 3, kh = w >> 2;
  const int c32 = lane & 31, hh = lane >> 5;
  const int xcd = b & 7, jj = b >> 3;
  const int rt = 4 * (xcd & 3) + (jj & 3), h = 8 * (xcd >> 2) + (jj >> 2);
  const int off = (28 * h) & 7, c0 = 28 * h - off;
  const int q4 = (lane & 15) >> 2, p4 = lane & 3, cb = (lane >> 4) & 1;
  const rsrc_t wsr = __builtin_amdgcn_make_buffer_rsrc(P.ws, 0, WS_END, 0x00020000);
  STAMP(0);
  const u64 ep = ld_flag(P.ws, WS_EB);
  {
    const int rsub = lane >> 3, p16 = lane & 7;
#pragma unroll
    for (int i = 0; i < 7; ++i) {
      int chunk = w + 8 * i + 14 * (jj & 3);
      chunk = chunk >= 56 ? chunk - 56 : chunk;
      const int row = 8 * chunk + rsub;
      const int seg = (p16 >> 2) ^ ((row >> 1) & 1);
      glds16(P.wqkv + (size_t)row * 1344 + 448 + 448 * seg + c0 + 8 * (p16 & 3), sm + B_W + chunk * 1024);
    }
  }
  bf16x8 xf[16];
  int lrot[4];
#pragma unroll
  for (int L = 0; L < 4; ++L) lrot[L] = kh ? (L + (jj >> 2)) % 3 : ((L + (jj >> 2)) & 3);
  const GAS bf16x8* xp = (const GAS bf16x8*)(P.x + (size_t)(128 * rt + 32 * wr + c32) * D_ + 8 * hh);
  {
    u32x4 raw[16];
    const GAS u32x4* xr = (const GAS u32x4*)(P.x + (size_t)(128 * rt + 32 * wr + (lane >> 3)) * D_ + 256 * kh + 8 * (lane & 7));
#pragma unroll
    for (int i = 0; i < 16; ++i)
      if (i < 12 || kh == 0) raw[i] = xr[(i & 3) * 8 * 56 + lrot[i >> 2] * 8];
    char* xs = sm + B_XS + w * 2048;
    const int cq = lane & 3, hsel = (lane >> 2) & 1;
    const int rsw = (c32 >> 2) & 3;
#pragma unroll
    for (int L = 0; L < 4; ++L) {
      if (L < 3 || kh == 0) {
#pragma unroll
        for (int hf = 0; hf < 2; ++hf) {
          if (hsel == hf) {
#pragma unroll
            for (int rg = 0; rg < 4; ++rg) {
              const int row = 8 * rg + (lane >> 3);
              *(u32x4*)(xs + row * 64 + ((cq ^ ((row >> 2) & 3)) * 16)) = raw[4 * L + rg];
            }
          }
          __builtin_amdgcn_fence(__ATOMIC_RELEASE, "wavefront"); __builtin_amdgcn_wave_barrier(); asm volatile("" ::: "memory");
#pragma unroll
          for (int tq = 0; tq < 2; ++tq)
            xf[4 * L + 2 * hf + tq] = *(const bf16x8*)(xs + c32 * 64 + (((2 * tq + hh) ^ rsw) * 16));
          asm volatile("" ::: "memory"); __builtin_amdgcn_wave_barrier();
        }
      }
    }
  }
  {
    const int c = (tid >> 4) - off, f0 = 2 * (tid & 15);
    const bool okc = (c >= 0) && (c < 28) && (f0 < 28);
    const unsigned short* wp = P.wphi + h * 784 + (okc ? c * 28 + f0 : 0);
    const unsigned v = (unsigned)wp[0] | ((unsigned)wp[1] << 16);
    *(unsigned*)(sm + B_WPH + tid * 4) = okc ? v : 0u;
    if (tid == 0) *(unsigned*)(sm + B_CNT) = 0u;
  }
  wait_vm0();
  const u64 tag = uniform64(ep) + 1ull;
  bar_lds();
  STAMP(1);
  f32x16 accK, accV;
#pragma unroll
  for (int r = 0; r < 16; ++r) { accK[r] = 0.f; accV[r] = 0.f; }
  {
    const int sw = (q4 >> 1) & 1;
    const char* kb = sm + B_W + (256 * kh + 8 * hh + q4) * 128 + (0 ^ sw) * 64 + 32 * cb + 8 * p4;
    const int vo = (sw ? -64 : 64);
    bf16x8 kf[2], vf[2];
    { const char* a = kb + lrot[0] * 8192; kf[0] = tr_frag(a, a + 512); vf[0] = tr_frag(a + vo, a + vo + 512); }
#pragma unroll
    for (int t = 0; t < 16; ++t) {
      if (t < 12 || kh == 0) {
        if (t + 1 < 12 || (t + 1 < 16 && kh == 0)) {
          const char* a = kb + lrot[(t + 1) >> 2] * 8192 + ((t + 1) & 3) * 2048;
          kf[(t + 1) & 1] = tr_frag(a, a + 512);
          vf[(t + 1) & 1] = tr_frag(a + vo, a + vo + 512);
        }
        accK = MFMA32(kf[t & 1], xf[t], accK);
        accV = MFMA32(xf[t], vf[t & 1], accV);
      }
      __builtin_amdgcn_sched_barrier(0);
    }
  }
  bar_lds();
  STAMP(2);
  const char* wpb = sm + B_WPH + (4 * hh + q4) * 64 + (16 * cb + 4 * p4) * 2;
  char* cslot = sm + B_COMB + wr * 8192;
  u32x4 phq_pos[2], phq_neg[2];
  if (kh == 1) {
    {
      const int rsub = lane >> 2, p16 = lane & 3;
#pragma unroll
      for (int i = 0; i < 7; ++i) {
        const int chunk = wr + 4 * i;
        glds16(P.wqkv + (size_t)(16 * chunk + rsub) * 1344 + c0 + 8 * p16, sm + B_W + chunk * 1024);
      }
    }
    acc_store(cslot, accK, lane); acc_store(cslot + 4096, accV, lane);
    bar_lds();
    __builtin_amdgcn_sched_barrier(0);
    const GAS bf16x8* xq = xp;
    asm volatile("" : "+v"(xq));
    bf16x8 xa[14], xb[14];
#pragma unroll
    for (int t = 0; t < 14; ++t) xa[t] = xq[2 * t];
    wait_vm0();
    bar_lds();
    f32x16 accQ;
#pragma unroll
    for (int r = 0; r < 16; ++r) accQ[r] = 0.f;
    const char* qb = sm + B_W + (8 * hh + q4) * 64 + 32 * cb + 8 * p4;
    bf16x8 qf[3];
    qf[0] = tr_frag(qb, qb + 256); qf[1] = tr_frag(qb + 1024, qb + 1024 + 256);
#pragma unroll
    for (int t = 0; t < 28; ++t) {
      if (t + 2 < 28) qf[(t + 2) % 3] = tr_frag(qb + (t + 2) * 1024, qb + (t + 2) * 1024 + 256);
      accQ = MFMA32(qf[t % 3], (t < 14) ? xa[t < 14 ? t : 0] : xb[t >= 14 ? t - 14 : 0], accQ);
      if (t == 1 || t == 6 || t == 11) {
#pragma unroll
        for (int i = t - 1; i < t + 4; ++i) if (i < 14) xb[i] = xq[28 + 2 * i];
      }
      __builtin_amdgcn_sched_barrier(0);
    }
    f32x16 pq;
#pragma unroll
    for (int r = 0; r < 16; ++r) pq[r] = 0.f;
    pq = MFMA32(tr_frag(wpb, wpb + 512), pack_acc(accQ, 0), pq);
    pq = MFMA32(tr_frag(wpb + 1024, wpb + 1536), pack_acc(accQ, 1), pq);
    dual_softmax(pq, hh, phq_pos, phq_neg);
  } else {
    bar_lds();
    acc_add(cslot, accK, lane); acc_add(cslot + 4096, accV, lane);
    f32x16 pk;
#pragma unroll
    for (int r = 0; r < 16; ++r) pk[r] = 0.f;
    pk = MFMA32(tr_frag(wpb, wpb + 512), pack_acc(accK, 0), pk);
    pk = MFMA32(tr_frag(wpb + 1024, wpb + 1536), pack_acc(accK, 1), pk);
    u32x4 kpos[2], kneg[2];
    dual_softmax(pk, hh, kpos, kneg);
    char* phw = sm + B_PHI + wr * B_PHIW;
#pragma unroll
    for (int gq = 0; gq < 4; ++gq) {
      *(u32x2*)(phw + c32 * 136 + (8 * gq + 4 * hh) * 2) = u32x2{kpos[gq >> 1][2 * (gq & 1)], kpos[gq >> 1][2 * (gq & 1) + 1]};
      *(u32x2*)(phw + c32 * 136 + 64 + (8 * gq + 4 * hh) * 2) = u32x2{kneg[gq >> 1][2 * (gq & 1)], kneg[gq >> 1][2 * (gq & 1) + 1]};
    }
    {
      const int cone = off ? 0 : 28;
      const bool valid = (c32 >= off) && (c32 < off + 28);
#pragma unroll
      for (int r = 0; r < 16; ++r) accV[r] = valid ? accV[r] : ((c32 == cone) ? 1.f : 0.f);
    }
    const bf16x8 vp0 = pack_acc(accV, 0), vp1 = pack_acc(accV, 1);
    f32x16 kvp[2];
#pragma unroll
    for (int m = 0; m < 2; ++m) {
#pragma unroll
      for (int r = 0; r < 16; ++r) kvp[m][r] = 0.f;
      const char* pb = phw + (4 * hh + q4) * 136 + (32 * m + 16 * cb + 4 * p4) * 2;
      const bf16x8 a0 = tr_frag(pb, pb + 8 * 136);
      const bf16x8 a1 = tr_frag(pb + 16 * 136, pb + 24 * 136);
      kvp[m] = MFMA32(a0, vp0, kvp[m]);
      kvp[m] = MFMA32(a1, vp1, kvp[m]);
    }
    acc_store(cslot, kvp[0], lane); acc_store(cslot + 4096, kvp[1], lane);
    bar_lds();
    {
      const unsigned base = WS_KVP + (unsigned)(h * 16 + rt) * 8192u;
#pragma unroll
      for (int i = 0; i < 2; ++i) {
        const int ck = 2 * wr + i;
        const char* q = sm + B_COMB + (ck * 64 + lane) * 16;
        const f32x4 v = (*(const f32x4*)(q) + *(const f32x4*)(q + 8192)) + (*(const f32x4*)(q + 16384) + *(const f32x4*)(q + 24576));
        st16_wt(wsr, base + (ck * 64 + lane) * 16, __builtin_bit_cast(u32x4, v));
      }
    }
    wait_vm0();
    if (lane == 0) {
      if (atomicAdd((unsigned*)(sm + B_CNT), 1u) == 3u) st_flag(P.ws, WS_FKV + 128 * h + 8 * rt, tag);
    }
#pragma unroll
    for (int s = 0; s < 2; ++s)
#pragma unroll
      for (int j = 0; j < 4; ++j) { phq_pos[s][j] = 0u; phq_neg[s][j] = 0u; }
  }
  STAMP(3);
  bar_lds();
  STAMP(4);
  const int rt2 = 4 * xcd + (jj & 3), ct = jj >> 2;
  for (int i = w; i < 49; i += 8) {
    const int id = i * 64 + lane;
    const int row = id / 7, c16 = id - 7 * row;
    glds16(P.wo + (size_t)row * D_ + 56 * ct + 8 * c16, sm + B_W + i * 1024);
  }
  if (tid < 16) *(unsigned*)(sm + B_W + 50176 + tid * 4) = 0u;
  if (w == 0) wait_flags(P.ws, WS_FKV + 128 * h, 16, tag);
  bar_lds();
  STAMP(5);
  {
    f32x4 s = {0.f, 0.f, 0.f, 0.f};
    u32x4 v[16];
#pragma unroll
    for (int r = 0; r < 16; ++r) v[r] = ld16_wt(wsr, WS_KVP + (unsigned)(h * 16 + r) * 8192u + tid * 16);
#pragma unroll
    for (int r = 0; r < 16; ++r) s += __builtin_bit_cast(f32x4, v[r]);
    const int m = tid >> 8, gq = (tid >> 6) & 3;
    const int f = 32 * m + 8 * gq + 4 * hh;
    unsigned short* kl = (unsigned short*)(sm + B_KVL);
    const unsigned p0 = pack_bf16(s[0], s[1]), p1 = pack_bf16(s[2], s[3]);
    kl[(f + 0) * 32 + c32] = (unsigned short)(p0 & 0xffffu);
    kl[(f + 1) * 32 + c32] = (unsigned short)(p0 >> 16);
    kl[(f + 2) * 32 + c32] = (unsigned short)(p1 & 0xffffu);
    kl[(f + 3) * 32 + c32] = (unsigned short)(p1 >> 16);
  }
  bar_lds();
  STAMP(6);
  if (kh == 1) {
    f32x16 num;
#pragma unroll
    for (int r = 0; r < 16; ++r) num[r] = 0.f;
#pragma unroll
    for (int m = 0; m < 2; ++m)
#pragma unroll
      for (int s = 0; s < 2; ++s) {
        const char* pb = sm + B_KVL + (32 * m + 16 * s + 4 * hh + q4) * 64 + (16 * cb + 4 * p4) * 2;
        const bf16x8 a = tr_frag(pb, pb + 8 * 64);
        num = MFMA32(a, as_frag(m == 0 ? phq_pos[s] : phq_neg[s]), num);
      }
    const float dsrc = off ? num[0] : num[12];
    float den = __shfl(dsrc, c32 + (off ? 0 : 32));
    den = bfr(bfr(den) + 1e-6f);
    const float rden = __builtin_amdgcn_rcpf(den);
    u32x2 ov[4];
#pragma unroll
    for (int gq = 0; gq < 4; ++gq) {
      const int d = 8 * gq + 4 * hh - off;
      const bool ok = (d >= 0) && (d < 28);
      ov[gq][0] = ok ? pack_bf16(bfr(num[4 * gq]) * rden, bfr(num[4 * gq + 1]) * rden) : 0u;
      ov[gq][1] = ok ? pack_bf16(bfr(num[4 * gq + 2]) * rden, bfr(num[4 * gq + 3]) * rden) : 0u;
    }
    const unsigned ob = WS_O + (unsigned)((4 * rt + wr) * 32 + 2 * h) * 1024u + lane * 16;
#pragma unroll
    for (int pr = 0; pr < 2; ++pr) {
      const auto r0 = __builtin_amdgcn_permlane32_swap(ov[2 * pr][0], ov[2 * pr + 1][0], false, false);
      const auto r1 = __builtin_amdgcn_permlane32_swap(ov[2 * pr][1], ov[2 * pr + 1][1], false, false);
      st16_wt(wsr, ob + pr * 1024, u32x4{r0[0], r1[0], r0[1], r1[1]});
    }
  }
  wait_vm0();
  bar_lds();
  if (tid == 0) st_flag(P.ws, WS_FO + 128 * rt + 8 * h, tag);
  STAMP(7);
  if (w == 0) wait_flags(P.ws, WS_FO + 128 * (rt2 >> 1), 16, tag);
  bar_lds();
  STAMP(8);
  {
    const int wr2 = w & 1, wc = (w >> 1) & 1;
    bf16x8 of[16];
    const unsigned ob = WS_O + (unsigned)((2 * rt2 + wr2) * 32 + 16 * kh) * 1024u + lane * 16;
#pragma unroll
    for (int t = 0; t < 16; ++t) of[t] = as_frag(ld16_wt(wsr, ob + 1024 * t));
    f32x16 acc;
#pragma unroll
    for (int r = 0; r < 16; ++r) acc[r] = 0.f;
    const char* wb = sm + B_W + (224 * kh + 8 * hh + q4) * 112 + (28 * wc + 16 * cb + 4 * p4) * 2;
#define WROW(t) ((((28 * ((t) >> 1)) & ~7) + 16 * ((t) & 1)) * 112)
    bf16x8 wf[3];
    wf[0] = tr_frag(wb + WROW(0), wb + WROW(0) + 448); wf[1] = tr_frag(wb + WROW(1), wb + WROW(1) + 448);
#pragma unroll
    for (int t = 0; t < 16; ++t) {
      if (t + 2 < 16) wf[(t + 2) % 3] = tr_frag(wb + WROW(t + 2), wb + WROW(t + 2) + 448);
      acc = MFMA32(wf[t % 3], of[t], acc);
      __builtin_amdgcn_sched_barrier(0);
    }
    STAMP(11);
    char* c2 = sm + B_CMB2 + (w & 3) * 4096;
    if (kh == 1) acc_store(c2, acc, lane);
    bar_lds();
    if (kh == 0) {
      acc_add(c2, acc, lane);
      unsigned short* dp = P.out + (size_t)(64 * rt2 + 32 * wr2 + c32) * OUTW + 448 + 56 * ct + 28 * wc;
#pragma unroll
      for (int gq = 0; gq < 4; ++gq) {
        const int c = 8 * gq + 4 * hh;
        if (c < 28) {
          u32x2 v;
          v[0] = pack_bf16(acc[4 * gq], acc[4 * gq + 1]);
          v[1] = pack_bf16(acc[4 * gq + 2], acc[4 * gq + 3]);
          *(u32x2*)(dp + c) = v;
        }
      }
    }
  }
  if (b == 0 && tid == 0) st_flag(P.ws, WS_EB, tag);
  STAMP(9);
}

__global__ void __launch_bounds__(512, 4) fused_kernel(Params p) {
  extern __shared__ __attribute__((aligned(16))) char sm[];
  const int b = blockIdx.x;
  if (b < 224) part_a(sm, p, b);
  else { __builtin_amdgcn_s_setprio(2); part_b(sm, p, b - 224); }
}

extern "C" void kernel_launch(void* const* d_in, const int* in_sizes, int n_in,
                              void* d_out, int out_size, void* d_ws, size_t ws_size,
                              hipStream_t stream) {
  Params p;
  p.u = (const unsigned short*)d_in[0];
  p.logA = (const unsigned short*)d_in[1];
  p.Aim = (const unsigned short*)d_in[2];
  p.Cre = (const unsigned short*)d_in[3];
  p.Cim = (const unsigned short*)d_in[4];
  p.logdt = (const unsigned short*)d_in[5];
  p.Dsk = (const unsigned short*)d_in[6];
  p.x = (const unsigned short*)d_in[7];
  p.wqkv = (const unsigned short*)d_in[8];
  p.wphi = (const unsigned short*)d_in[9];
  p.wo = (const unsigned short*)d_in[10];
  p.out = (unsigned short*)d_out;
  p.ws = (char*)d_ws;
  (void)hipFuncSetAttribute((const void*)fused_kernel, hipFuncAttributeMaxDynamicSharedMemorySize, LDS_BYTES);
  fused_kernel<<<480, 512, LDS_BYTES, stream>>>(p);
}
```

```cpp
#include <hip/hip_runtime.h>
#include <hip/hip_bf16.h>
#include <stdint.h>

using bf16x8 = __attribute__((ext_vector_type(8))) short;
using s16x4  = __attribute__((ext_vector_type(4))) short;
using f32x16 = __attribute__((ext_vector_type(16))) float;
using f32x4  = __attribute__((ext_vector_type(4))) float;
using u32x4  = __attribute__((ext_vector_type(4))) unsigned;
using u32x2  = __attribute__((ext_vector_type(2))) unsigned;
typedef __bf16 bfx2_t __attribute__((ext_vector_type(2)));
typedef float  f32x2  __attribute__((ext_vector_type(2)));
typedef unsigned long long u64;

#define DI __device__ __forceinline__
#define LAS __attribute__((address_space(3)))
#define GAS __attribute__((address_space(1)))
#define MFMA32(a, b, c) __builtin_amdgcn_mfma_f32_32x32x16_bf16((a), (b), (c), 0, 0, 0)
#define MFMAF(a, b, c) __builtin_amdgcn_mfma_f32_16x16x4f32((a), (b), (c), 0, 0, 0)
#define MFMA16(a, b, c) __builtin_amdgcn_mfma_f32_16x16x32_bf16((a), (b), (c), 0, 0, 0)

constexpr int D_  = 448;
constexpr int N_  = 256;
constexpr int OUTW = 896;

#define STAMP(i)
#define WSTAMP(i)

constexpr unsigned WS_EB  = 0;
constexpr unsigned WS_EA  = 256;
constexpr unsigned WS_FKV = 4096;
constexpr unsigned WS_FO  = 8192;
constexpr unsigned WS_FUT = 12288;
constexpr unsigned WS_KVP = 1u << 20;
constexpr unsigned WS_O   = 4u << 20;
constexpr unsigned WS_UT  = 8u << 20;
constexpr unsigned WS_END = 10u << 20;

DI unsigned pack_bf16(float lo, float hi) {
  f32x2 v = {lo, hi};
  return __builtin_bit_cast(unsigned, __builtin_convertvector(v, bfx2_t));
}
DI float bf_lo(unsigned p) { return __uint_as_float(p << 16); }
DI float bf_hi(unsigned p) { return __uint_as_float(p & 0xffff0000u); }
DI float bf2f(unsigned short b) { return __uint_as_float(((unsigned)b) << 16); }
DI float bfr(float x) { return bf_lo(pack_bf16(x, 0.f)); }
DI s16x4 tr_read(const char* p) {
  return __builtin_amdgcn_ds_read_tr16_b64_v4i16((LAS s16x4*)(p));
}
DI bf16x8 tr_frag(const char* p0, const char* p1) {
  s16x4 a = tr_read(p0), b = tr_read(p1);
  return __builtin_shufflevector(a, b, 0, 1, 2, 3, 4, 5, 6, 7);
}
DI bf16x8 as_frag(u32x4 v) { return __builtin_bit_cast(bf16x8, v); }
DI void glds16(const void* g, void* lds_wave_base) {
  __builtin_amdgcn_global_load_lds((const unsigned*)g, (unsigned*)lds_wave_base, 16, 0, 0);
}
DI void wait_vm0() { asm volatile("s_waitcnt vmcnt(0)" ::: "memory"); }
DI void bar_lds() { asm volatile("s_waitcnt lgkmcnt(0)" ::: "memory"); __builtin_amdgcn_s_barrier(); asm volatile("" ::: "memory"); }

typedef __amdgpu_buffer_rsrc_t rsrc_t;
DI void st16_wt(rsrc_t r, unsigned off, u32x4 v) { __builtin_amdgcn_raw_buffer_store_b128(v, r, off, 0, 16); }
DI void st8_wt(rsrc_t r, unsigned off, u32x2 v) { __builtin_amdgcn_raw_buffer_store_b64(v, r, off, 0, 16); }
DI u32x4 ld16_wt(rsrc_t r, unsigned off) { return __builtin_amdgcn_raw_buffer_load_b128(r, off, 0, 16); }
DI unsigned ld4_wt(rsrc_t r, unsigned off) { return __builtin_amdgcn_raw_buffer_load_b32(r, off, 0, 16); }
DI u64 ld_flag(const char* ws, unsigned off) {
  return __hip_atomic_load((GAS u64*)(ws + off), __ATOMIC_RELAXED, __HIP_MEMORY_SCOPE_AGENT);
}
DI void st_flag(char* ws, unsigned off, u64 v) {
  __hip_atomic_store((GAS u64*)(ws + off), v, __ATOMIC_RELAXED, __HIP_MEMORY_SCOPE_AGENT);
}
DI void wait_flags(const char* ws, unsigned off, int n, u64 tag) {
  const int lane = threadIdx.x & 63;
  for (int it = 0; it < (1 << 16); ++it) {
    const u64 v = (lane < n) ? ld_flag(ws, off + 8 * lane) : tag;
    if (__all(v == tag)) break;
    __builtin_amdgcn_s_sleep(1);
  }
  asm volatile("" ::: "memory");
}
DI u64 uniform64(u64 v) {
  const unsigned lo = __builtin_amdgcn_readfirstlane((unsigned)v), hi = __builtin_amdgcn_readfirstlane((unsigned)(v >> 32));
  return ((u64)hi << 32) | lo;
}

struct Params {
  const unsigned short *u, *logA, *Aim, *Cre, *Cim, *logdt, *Dsk, *x, *wqkv, *wphi, *wo;
  unsigned short* out;
  char* ws;
};

constexpr int LDS_BYTES = 80 * 1024;

constexpr int A_UCOPY = 4160;
constexpr int A_US    = 0;
constexpr int A_CH0   = 4 * A_UCOPY;
constexpr int A_KRH   = 0;
constexpr int A_KRL   = 4576;
constexpr int A_KD    = 9152;
constexpr int A_PR2   = A_KD + 4112;
constexpr int A_RED   = A_PR2 + 2048;
constexpr int A_SUM   = A_RED + 16384;
constexpr int A_CHS   = A_SUM + 64;
constexpr int A_CNT   = A_CH0 + 2 * A_CHS;
static_assert(A_CNT + 16 <= LDS_BYTES, "A lds");

DI void cexp_eval(float lr, float li, float t, float& er, float& ei) {
  const float xr = lr * t;
  const float e  = __builtin_amdgcn_exp2f(xr * 1.44269504088896340736f);
  const float x  = li * t;
  const float C_HI = 0.15915494309189533577f;
  const float C_LO = (float)(0.15915494309189533577 - (double)0.15915494309189533577f);
  const float rh = x * C_HI;
  float rl = __builtin_fmaf(x, C_HI, -rh);
  rl = __builtin_fmaf(x, C_LO, rl);
  const float rev = (rh - __builtin_rintf(rh)) + rl;
  er = e * __builtin_amdgcn_cosf(rev);
  ei = e * __builtin_amdgcn_sinf(rev);
}
DI void cexp_fast(const f32x4& p, float t, float& er, float& ei) {
  const float e = __builtin_amdgcn_exp2f(p[0] * t);
  const float rev = __builtin_fmaf(t, p[2], __builtin_amdgcn_fractf(t * p[1]));
  er = e * __builtin_amdgcn_cosf(rev);
  ei = e * __builtin_amdgcn_sinf(rev);
}
DI void split2(float a, float b, unsigned& hi, unsigned& lo) {
  hi = pack_bf16(a, b);
  lo = pack_bf16(a - bf_lo(hi), b - bf_hi(hi));
}
DI void split2v(const f32x2& R, const f32x2& I, u32x4& h, u32x4& l, int q, unsigned mlo, unsigned mhi) {
  const unsigned h0 = pack_bf16(R[0], I[0]), h1 = pack_bf16(R[1], I[1]);
  h[2 * q] = h0; h[2 * q + 1] = h1;
  float r0, i0, r1, i1;
  asm("v_dot2_f32_bf16 %0, %4, %6, %8\n\tv_dot2_f32_bf16 %1, %4, %7, %9\n\t"
      "v_dot2_f32_bf16 %2, %5, %6, %10\n\tv_dot2_f32_bf16 %3, %5, %7, %11\n\ts_nop 2"
      : "=&v"(r0), "=&v"(i0), "=&v"(r1), "=&v"(i1)
      : "v"(h0), "v"(h1), "s"(mlo), "s"(mhi), "v"(R[0]), "v"(I[0]), "v"(R[1]), "v"(I[1]));
  l[2 * q] = pack_bf16(r0, i0);
  l[2 * q + 1] = pack_bf16(r1, i1);
}
DI void bar4(char* cnt, unsigned& target) {
  asm volatile("s_waitcnt lgkmcnt(0)" ::: "memory");
  target += 4u;
  if ((threadIdx.x & 63) == 0) __hip_atomic_fetch_add((LAS unsigned*)cnt, 1u, __ATOMIC_RELAXED, __HIP_MEMORY_SCOPE_WORKGROUP);
  while ((unsigned)__builtin_amdgcn_readfirstlane(__hip_atomic_load((LAS unsigned*)cnt, __ATOMIC_RELAXED, __HIP_MEMORY_SCOPE_WORKGROUP)) < target)
    __builtin_amdgcn_s_sleep(1);
  asm volatile("" ::: "memory");
}

constexpr int bf16_round_int(int t) {
  if (t < 256) return t;
  int sh = 0;
  while ((t >> sh) >= 256) ++sh;
  const int q = t >> sh, r = t & ((1 << sh) - 1), half = 1 << (sh - 1);
  return (q + ((r > half || (r == half && (q & 1))) ? 1 : 0)) << sh;
}
constexpr int kd_slot(int tap) {
  if (tap < 0) return 1025;
  const int p = bf16_round_int(tap);
  if (p < 256) return p;
  if (p >= 2048) return 1024;
  int s = 0;
  while ((p >> s) >= 256) ++s;
  return s * 256 + (p >> s);
}
struct KTab { unsigned short v[2304]; };
constexpr KTab make_ktab() {
  KTab t{};
  for (int i = 0; i < 2304; ++i) t.v[i] = (unsigned short)(4 * kd_slot(2047 - i));
  return t;
}
__device__ __attribute__((aligned(16))) const KTab KTAB = make_ktab();

DI void part_a(char* sm, const Params& P, int a) {
  const int tid = threadIdx.x, lane = tid & 63, w = __builtin_amdgcn_readfirstlane(tid >> 6);
  const int ch = w >> 2, cw = w & 3, ctid = tid & 255;
  const int i16 = lane & 15, g = lane >> 4;
  const int d0 = 56 * (a & 7) + 2 * (a >> 3);
  const int cg = d0 >> 6, r32 = (d0 & 63) >> 1;
  const int d = d0 + ch;
  char* cs = sm + A_CH0 + ch * A_CHS;
  const rsrc_t wsr = __builtin_amdgcn_make_buffer_rsrc(P.ws, 0, WS_END, 0x00020000);
  STAMP(0);
  const u64 ep = ld_flag(P.ws, WS_EA + 256 * cg);
  const unsigned short pla = P.logA[d * N_ + ctid], pai = P.Aim[d * N_ + ctid];
  const unsigned short pcr = P.Cre[d * N_ + ctid], pci = P.Cim[d * N_ + ctid];
  const unsigned short pdt = P.logdt[d];
  const u32x4 ut = *(const u32x4*)(P.u + (size_t)(64 * r32 + (tid >> 3)) * D_ + 64 * cg + 8 * (tid & 7));
  {
    const float dt = bfr(expf(bf2f(pdt)));
    const float am = bfr(expf(bf2f(pla)));
    const float lr = -am * dt, li = bf2f(pai) * dt;
    const float cr = bf2f(pcr) * dt, ci = bf2f(pci) * dt;
    {
      const float C_HI = 0.15915494309189533577f;
      const float C_LO = (float)(0.15915494309189533577 - (double)0.15915494309189533577f);
      const float q = li * C_HI;
      const float er = __builtin_fmaf(li, C_LO, __builtin_fmaf(li, C_HI, -q));
      const float a16 = __uint_as_float(__float_as_uint(q) & 0xffffff00u);
      *(f32x4*)(cs + A_KD + ctid * 16) = f32x4{lr * 1.44269504088896340736f, a16, (q - a16) + er, 0.f};
      *(f32x2*)(cs + A_PR2 + ctid * 8) = f32x2{cr, ci};
    }
    float er, ei;
    cexp_eval(lr, li, 2048.f, er, ei);
    float t = cr * er - ci * ei;
#pragma unroll
    for (int o = 32; o >= 1; o >>= 1) t += __shfl_xor(t, o);
    if (lane == 0) ((float*)(cs + A_SUM))[cw] = t;
  }
  const u64 tag = uniform64(ep) + 1ull;
  {
    unsigned short* T = (unsigned short*)(sm + A_US);
    const int row = tid >> 3, c8 = 8 * (tid & 7);
#pragma unroll
    for (int j = 0; j < 4; ++j) {
      T[(c8 + 2 * j) * 72 + row] = (unsigned short)(ut[j] & 0xffffu);
      T[(c8 + 2 * j + 1) * 72 + row] = (unsigned short)(ut[j] >> 16);
    }
    if (tid < 3) *(unsigned*)(sm + A_CNT + 4 * tid) = 0u;
    bar_lds();
    if (w < 4) {
#pragma unroll
      for (int i = 0; i < 2; ++i) {
        const int c = (tid >> 3) + 32 * i;
        const u32x4 v = *(const u32x4*)(T + c * 72 + 8 * (tid & 7));
        st16_wt(wsr, WS_UT + ((64 * cg + c) * 2048 + 64 * r32 + 8 * (tid & 7)) * 2, v);
      }
    }
  }
  STAMP(1);
  u32x4 uv, kt0, kt1; unsigned unx = 0u;
  char* cb4 = sm + A_CNT + 4 + 4 * ch;
  unsigned b4t = 0u;
  {
    f32x4 acc[4];
#pragma unroll
    for (int s = 0; s < 4; ++s) acc[s] = f32x4{0.f, 0.f, 0.f, 0.f};
    const float ty = (float)i16, tx = (float)(16 * i16);
    unsigned mlo = 0x0000bf80u, mhi = 0xbf800000u;
    asm volatile("" : "+s"(mlo), "+s"(mhi));
    WSTAMP(0);
#pragma unroll 1
    for (int ks = 0; ks < 4; ++ks) {
      if (ks == 3) WSTAMP(1);
      if (ks == 1 && w < 4) {
        wait_vm0();
        if (lane == 0 && atomicAdd((unsigned*)(sm + A_CNT), 1u) == 3u) st_flag(P.ws, WS_FUT + 256 * cg + 8 * r32, tag);
      }
      if (ks == 3) {
        wait_flags(P.ws, WS_FUT + 256 * cg, 32, tag);
        uv = ld16_wt(wsr, WS_UT + (d * 2048 + 8 * ctid) * 2);
        if (ctid < 255) unx = ld4_wt(wsr, WS_UT + (d * 2048 + 8 * ctid + 8) * 2);
        kt0 = *(const u32x4*)(KTAB.v + 8 * ctid);
        kt1 = *(const u32x4*)(KTAB.v + 2048 + 8 * (ctid & 31));
      }
      f32x2 YR[2], YI[2], XR[2], XI[2], CR[2], CI[2];
      const char* pp = cs + A_KD + (64 * cw + 16 * ks + 4 * g) * 16;
      const char* pc = cs + A_PR2 + (64 * cw + 16 * ks + 4 * g) * 8;
#pragma unroll
      for (int m = 0; m < 4; ++m) {
        const f32x4 p = *(const f32x4*)(pp + m * 16);
        float r0, i0, r1, i1;
        cexp_fast(p, ty, r0, i0);
        cexp_fast(p, tx, r1, i1);
        YR[m >> 1][m & 1] = r0; YI[m >> 1][m & 1] = i0;
        XR[m >> 1][m & 1] = r1; XI[m >> 1][m & 1] = i1;
      }
#pragma unroll
      for (int q = 0; q < 2; ++q) {
        const f32x4 c = *(const f32x4*)(pc + q * 16);
        CR[q] = f32x2{c[0], c[2]}; CI[q] = f32x2{c[1], c[3]};
      }
#pragma unroll
      for (int s = 0; s < 4; ++s) {
        u32x4 ah, al, bh, bl;
#pragma unroll
        for (int q = 0; q < 2; ++q) {
          const f32x2 zr = CR[q] * XR[q] - CI[q] * XI[q];
          const f32x2 zn = -(CR[q] * XI[q]) - CI[q] * XR[q];
          split2v(YR[q], YI[q], ah, al, q, mlo, mhi);
          split2v(zr, zn, bh, bl, q, mlo, mhi);
        }
        acc[s] = MFMA16(as_frag(ah), as_frag(bh), acc[s]);
        acc[s] = MFMA16(as_frag(ah), as_frag(bl), acc[s]);
        acc[s] = MFMA16(as_frag(al), as_frag(bh), acc[s]);
        if (s < 3) {
#pragma unroll
          for (int q = 0; q < 2; ++q) {
            const f32x2 a = YR[q], b = YI[q], c = XR[q], e = XI[q];
            YR[q] = a * a - b * b; YI[q] = (a + a) * b;
            XR[q] = c * c - e * e; XI[q] = (c + c) * e;
          }
        }
      }
    }
    WSTAMP(2);
#pragma unroll
    for (int s = 0; s < 4; ++s)
      *(f32x4*)(cs + A_RED + ((cw * 4 + s) * 256 + 16 * i16 + 4 * g) * 4) = acc[s];
  }
  bar4(cb4, b4t);
  STAMP(2);
  {
    const float* red = (const float*)(cs + A_RED);
    unsigned* kp = (unsigned*)(cs + A_KD);
#pragma unroll
    for (int s = 0; s < 4; ++s) {
      const float v = 2.f * ((red[(0 * 4 + s) * 256 + ctid] + red[(1 * 4 + s) * 256 + ctid]) +
                             (red[(2 * 4 + s) * 256 + ctid] + red[(3 * 4 + s) * 256 + ctid]));
      const float vh = bfr(v);
      kp[s * 256 + ctid] = pack_bf16(vh, v - vh);
    }
    if (ctid == 0) {
      const float* sp = (const float*)(cs + A_SUM);
      const float v = 2.f * ((sp[0] + sp[1]) + (sp[2] + sp[3]));
      const float vh = bfr(v);
      kp[1024] = pack_bf16(vh, v - vh);
      kp[1025] = 0u;
    }
  }
  bar4(cb4, b4t);
  {
    const char* kp = cs + A_KD;
#pragma unroll
    for (int part = 0; part < 2; ++part) {
      if (part == 0 || ctid < 30) {
        const u32x4 kt = part ? kt1 : kt0;
        u32x4 h, l;
#pragma unroll
        for (int j = 0; j < 4; ++j) {
          const unsigned a = *(const unsigned*)(kp + (kt[j] & 0xffffu));
          const unsigned b = *(const unsigned*)(kp + (kt[j] >> 16));
          h[j] = __builtin_amdgcn_perm(b, a, 0x05040100u);
          l[j] = __builtin_amdgcn_perm(b, a, 0x07060302u);
        }
        const int c = ctid + 256 * part;
        *(u32x4*)(cs + A_KRH + c * 16) = h;
        *(u32x4*)(cs + A_KRL + c * 16) = l;
      }
    }
  }
  {
    char* c0p = sm + A_US + (2 * ch) * A_UCOPY;
    char* c1p = c0p + A_UCOPY;
    *(u32x4*)(c0p + (8 * ctid + 16) * 2) = uv;
    u32x4 sh;
    sh[0] = (uv[0] >> 16) | (uv[1] << 16); sh[1] = (uv[1] >> 16) | (uv[2] << 16);
    sh[2] = (uv[2] >> 16) | (uv[3] << 16); sh[3] = (uv[3] >> 16) | (unx << 16);
    *(u32x4*)(c1p + (8 * ctid + 16) * 2) = sh;
    if (ctid == 0) {
      *(u32x4*)(c0p) = u32x4{0u, 0u, 0u, 0u}; *(u32x4*)(c0p + 16) = u32x4{0u, 0u, 0u, 0u};
      *(u32x4*)(c1p) = u32x4{0u, 0u, 0u, 0u}; *(u32x4*)(c1p + 16) = u32x4{0u, 0u, 0u, uv[0] << 16};
    }
  }
  bar4(cb4, b4t);
  STAMP(3);
  f32x4 acc[8];
  {
#pragma unroll
    for (int q = 0; q < 8; ++q) acc[q] = f32x4{0.f, 0.f, 0.f, 0.f};
    const int par = (i16 + 1) & 1;
    const char* ubase = sm + A_US + (2 * ch + par) * A_UCOPY + 2 * (i16 + 1 - par + 8 * g);
#pragma unroll 1
    for (int cc = 0; cc < 2; ++cc) {
      const int r = 2 * cw + cc;
      bf16x8 af[8];
#pragma unroll
      for (int q = 0; q < 8; ++q) {
        const unsigned* p = (const unsigned*)(ubase + 64 * (r + 8 * q));
        u32x4 v; v[0] = p[0]; v[1] = p[1]; v[2] = p[2]; v[3] = p[3];
        af[q] = as_frag(v);
      }
      const int kb = 2 * (2032 - 16 * i16 + 32 * r + 8 * g);
#pragma unroll
      for (int b = 0; b < 8; ++b) {
        const bf16x8 bh = *(const bf16x8*)(cs + A_KRH + kb - 512 * b);
        const bf16x8 bl = *(const bf16x8*)(cs + A_KRL + kb - 512 * b);
#pragma unroll
        for (int q = 0; q + b < 8; ++q) acc[q + b] = MFMA16(af[q], bh, acc[q + b]);
#pragma unroll
        for (int q = 0; q + b < 8; ++q) acc[q + b] = MFMA16(af[q], bl, acc[q + b]);
      }
    }
  }
  STAMP(4);
  const unsigned short pds = P.Dsk[d];
  f32x4 y2[2];
  {
    const bool up = (cw & 2) != 0;
    f32x4 keep[4];
#pragma unroll
    for (int i = 0; i < 4; ++i) {
      keep[i] = up ? acc[4 + i] : acc[i];
      const f32x4 snd = up ? acc[i] : acc[4 + i];
      *(f32x4*)(cs + A_RED + ((cw * 4 + i) * 64 + lane) * 16) = snd;
    }
    bar4(cb4, b4t);
#pragma unroll
    for (int i = 0; i < 4; ++i) keep[i] += *(const f32x4*)(cs + A_RED + (((cw ^ 2) * 4 + i) * 64 + lane) * 16);
    const bool od = (cw & 1) != 0;
#pragma unroll
    for (int i = 0; i < 2; ++i) {
      y2[i] = od ? keep[2 + i] : keep[i];
      const f32x4 snd = od ? keep[i] : keep[2 + i];
      *(f32x4*)(cs + A_KRH + ((cw * 2 + i) * 64 + lane) * 16) = snd;
    }
    bar4(cb4, b4t);
#pragma unroll
    for (int i = 0; i < 2; ++i) y2[i] += *(const f32x4*)(cs + A_KRH + (((cw ^ 1) * 2 + i) * 64 + lane) * 16);
  }
  {
    const float ds = bf2f(pds);
    const unsigned short* uc = (const unsigned short*)(sm + A_US + (2 * ch) * A_UCOPY) + 16;
#pragma unroll
    for (int i = 0; i < 2; ++i) {
      const int l = 256 * (4 * (cw >> 1) + 2 * (cw & 1) + i) + 16 * i16 + 4 * g;
      const u32x2 uu = *(const u32x2*)(uc + l);
      u32x2 o;
      o[0] = pack_bf16(bfr(y2[i][0]) + bfr(bf_lo(uu[0]) * ds), bfr(y2[i][1]) + bfr(bf_hi(uu[0]) * ds));
      o[1] = pack_bf16(bfr(y2[i][2]) + bfr(bf_lo(uu[1]) * ds), bfr(y2[i][3]) + bfr(bf_hi(uu[1]) * ds));
      *(u32x2*)(cs + A_KD + l * 2) = o;
    }
  }
  bar_lds();
  {
    const unsigned short* o0 = (const unsigned short*)(sm + A_CH0 + A_KD);
    const unsigned short* o1 = (const unsigned short*)(sm + A_CH0 + A_CHS + A_KD);
#pragma unroll
    for (int i = 0; i < 4; ++i) {
      const int l = tid + 512 * i;
      *(unsigned*)(P.out + (size_t)l * OUTW + d0) = (unsigned)o0[l] | ((unsigned)o1[l] << 16);
    }
  }
  if (r32 == 0 && tid == 0) st_flag(P.ws, WS_EA + 256 * cg, tag);
  STAMP(5);
}

DI bf16x8 pack_acc(const f32x16& x, int s) {
  u32x4 p;
#pragma unroll
  for (int j = 0; j < 4; ++j) p[j] = pack_bf16(x[8 * s + 2 * j], x[8 * s + 2 * j + 1]);
  return as_frag(p);
}

DI void dual_softmax(const f32x16& pacc, int hh, u32x4 (&pos)[2], u32x4 (&neg)[2]) {
  f32x2 p[8];
#pragma unroll
  for (int k = 0; k < 8; ++k) {
    const unsigned b = pack_bf16(pacc[2 * k], pacc[2 * k + 1]);
    p[k] = f32x2{bf_lo(b), bf_hi(b)};
  }
  float mx = fmaxf(p[0][0], p[0][1]), mn = fminf(p[0][0], p[0][1]);
#pragma unroll
  for (int k = 1; k < 6; ++k) { mx = fmaxf(fmaxf(mx, p[k][0]), p[k][1]); mn = fminf(fminf(mn, p[k][0]), p[k][1]); }
  {
    const float m2 = fmaxf(fmaxf(p[6][0], p[6][1]), fmaxf(p[7][0], p[7][1]));
    const float n2 = fminf(fminf(p[6][0], p[6][1]), fminf(p[7][0], p[7][1]));
    mx = hh ? mx : fmaxf(mx, m2); mn = hh ? mn : fminf(mn, n2);
  }
  mx = fmaxf(mx, __shfl_xor(mx, 32));
  mn = fminf(mn, __shfl_xor(mn, 32));
  const float L2E = 1.44269504088896340736f;
  const f32x2 mxl = {mx * L2E, mx * L2E}, mnl = {mn * L2E, mn * L2E};
  f32x2 ep[8], en[8], sp2 = {0.f, 0.f}, sn2 = {0.f, 0.f};
#pragma unroll
  for (int k = 0; k < 8; ++k) {
    const f32x2 a = p[k] * L2E;
    const f32x2 x = a - mxl, y = mnl - a;
    ep[k] = f32x2{__builtin_amdgcn_exp2f(x[0]), __builtin_amdgcn_exp2f(x[1])};
    en[k] = f32x2{__builtin_amdgcn_exp2f(y[0]), __builtin_amdgcn_exp2f(y[1])};
    if (k >= 6) { ep[k] = hh ? f32x2{0.f, 0.f} : ep[k]; en[k] = hh ? f32x2{0.f, 0.f} : en[k]; }
    sp2 += ep[k]; sn2 += en[k];
  }
  float sp = sp2[0] + sp2[1], sn = sn2[0] + sn2[1];
  sp += __shfl_xor(sp, 32);
  sn += __shfl_xor(sn, 32);
  const float rp = __builtin_amdgcn_rcpf(sp), rn = __builtin_amdgcn_rcpf(sn);
#pragma unroll
  for (int k = 0; k < 8; ++k) {
    const f32x2 a = ep[k] * rp, b = en[k] * rn;
    pos[k >> 2][k & 3] = pack_bf16(a[0], a[1]);
    neg[k >> 2][k & 3] = pack_bf16(b[0], b[1]);
  }
}
DI void acc_store(char* p, const f32x16& a, int lane) {
#pragma unroll
  for (int gq = 0; gq < 4; ++gq)
    *(f32x4*)(p + (gq * 64 + lane) * 16) = f32x4{a[4 * gq], a[4 * gq + 1], a[4 * gq + 2], a[4 * gq + 3]};
}
DI void acc_add(const char* p, f32x16& a, int lane) {
#pragma unroll
  for (int gq = 0; gq < 4; ++gq) {
    const f32x4 v = *(const f32x4*)(p + (gq * 64 + lane) * 16);
    a[4 * gq] += v[0]; a[4 * gq + 1] += v[1]; a[4 * gq + 2] += v[2]; a[4 * gq + 3] += v[3];
  }
}

constexpr int B_W    = 0;
constexpr int B_COMB = 28672;
constexpr int B_PHI  = 61440;
constexpr int B_PHIW = 32 * 136;
constexpr int B_KVL  = 61440;
constexpr int B_WPH  = 79872;
constexpr int B_XS   = 57344;
constexpr int B_CNT  = 79360;
constexpr int B_CMB2 = 53248;

DI void part_b(char* sm, const Params& P, int b) {
  const int tid = threadIdx.x, lane = tid & 63, w = __builtin_amdgcn_readfirstlane(tid >> 6);
  const int wr = w & 3, kh = w >> 2;
  const int c32 = lane & 31, hh = lane >> 5;
  const int xcd = b & 7, jj = b >> 3;
  const int rt = 4 * (xcd & 3) + (jj & 3), h = 8 * (xcd >> 2) + (jj >> 2);
  const int off = (28 * h) & 7, c0 = 28 * h - off;
  const int q4 = (lane & 15) >> 2, p4 = lane & 3, cb = (lane >> 4) & 1;
  const rsrc_t wsr = __builtin_amdgcn_make_buffer_rsrc(P.ws, 0, WS_END, 0x00020000);
  STAMP(0);
  const u64 ep = ld_flag(P.ws, WS_EB);
  {
    const int rsub = lane >> 3, p16 = lane & 7;
#pragma unroll
    for (int i = 0; i < 7; ++i) {
      int chunk = w + 8 * i + 14 * (jj & 3);
      chunk = chunk >= 56 ? chunk - 56 : chunk;
      const int row = 8 * chunk + rsub;
      const int seg = (p16 >> 2) ^ ((row >> 1) & 1);
      glds16(P.wqkv + (size_t)row * 1344 + 448 + 448 * seg + c0 + 8 * (p16 & 3), sm + B_W + chunk * 1024);
    }
  }
  bf16x8 xf[16];
  int lrot[4];
#pragma unroll
  for (int L = 0; L < 4; ++L) lrot[L] = kh ? (L + (jj >> 2)) % 3 : ((L + (jj >> 2)) & 3);
  const GAS bf16x8* xp = (const GAS bf16x8*)(P.x + (size_t)(128 * rt + 32 * wr + c32) * D_ + 8 * hh);
  {
    u32x4 raw[16];
    const GAS u32x4* xr = (const GAS u32x4*)(P.x + (size_t)(128 * rt + 32 * wr + (lane >> 3)) * D_ + 256 * kh + 8 * (lane & 7));
#pragma unroll
    for (int i = 0; i < 16; ++i)
      if (i < 12 || kh == 0) raw[i] = xr[(i & 3) * 8 * 56 + lrot[i >> 2] * 8];
    char* xs = sm + B_XS + w * 2048;
    const int cq = lane & 3, hsel = (lane >> 2) & 1, r8 = lane >> 3;
    char* wa0 = xs + r8 * 64 + ((cq ^ (r8 >> 2)) * 16);
    char* wa1 = xs + 512 + r8 * 64 + ((cq ^ (2 + (r8 >> 2))) * 16);
    const char* ra0 = xs + c32 * 64 + ((hh ^ ((c32 >> 2) & 3)) * 16);
    const char* ra1 = xs + c32 * 64 + (((2 + hh) ^ ((c32 >> 2) & 3)) * 16);
#pragma unroll
    for (int L = 0; L < 4; ++L) {
      if (L < 3 || kh == 0) {
#pragma unroll
        for (int hf = 0; hf < 2; ++hf) {
          if (hsel == hf) {
            *(u32x4*)(wa0) = raw[4 * L]; *(u32x4*)(wa1) = raw[4 * L + 1];
            *(u32x4*)(wa0 + 1024) = raw[4 * L + 2]; *(u32x4*)(wa1 + 1024) = raw[4 * L + 3];
          }
          __builtin_amdgcn_fence(__ATOMIC_RELEASE, "wavefront"); __builtin_amdgcn_wave_barrier(); asm volatile("" ::: "memory");
          xf[4 * L + 2 * hf] = *(const bf16x8*)(ra0);
          xf[4 * L + 2 * hf + 1] = *(const bf16x8*)(ra1);
          asm volatile("" ::: "memory"); __builtin_amdgcn_wave_barrier();
        }
      }
    }
  }
  {
    const int c = (tid >> 4) - off, f0 = 2 * (tid & 15);
    const bool okc = (c >= 0) && (c < 28) && (f0 < 28);
    const unsigned short* wp = P.wphi + h * 784 + (okc ? c * 28 + f0 : 0);
    const unsigned v = (unsigned)wp[0] | ((unsigned)wp[1] << 16);
    *(unsigned*)(sm + B_WPH + tid * 4) = okc ? v : 0u;
    if (tid < 3) *(unsigned*)(sm + B_CNT + 4 * tid) = 0u;
  }
  wait_vm0();
  const u64 tag = uniform64(ep) + 1ull;
  bar_lds();
  __builtin_amdgcn_s_setprio(2);
  STAMP(1);
  f32x16 accK, accV;
#pragma unroll
  for (int r = 0; r < 16; ++r) { accK[r] = 0.f; accV[r] = 0.f; }
  {
    const int sw = (q4 >> 1) & 1;
    const char* kb = sm + B_W + (256 * kh + 8 * hh + q4) * 128 + (0 ^ sw) * 64 + 32 * cb + 8 * p4;
    const int vo = (sw ? -64 : 64);
    bf16x8 kf[2], vf[2];
    { const char* a = kb + lrot[0] * 8192; kf[0] = tr_frag(a, a + 512); vf[0] = tr_frag(a + vo, a + vo + 512); }
#pragma unroll
    for (int t = 0; t < 16; ++t) {
      if (t < 12 || kh == 0) {
        if (t + 1 < 12 || (t + 1 < 16 && kh == 0)) {
          const char* a = kb + lrot[(t + 1) >> 2] * 8192 + ((t + 1) & 3) * 2048;
          kf[(t + 1) & 1] = tr_frag(a, a + 512);
          vf[(t + 1) & 1] = tr_frag(a + vo, a + vo + 512);
        }
        accK = MFMA32(kf[t & 1], xf[t], accK);
        accV = MFMA32(xf[t], vf[t & 1], accV);
      }
      __builtin_amdgcn_sched_barrier(0);
    }
  }
  bar_lds();
  STAMP(2);
  const char* wpb = sm + B_WPH + (4 * hh + q4) * 64 + (16 * cb + 4 * p4) * 2;
  char* cslot = sm + B_COMB + wr * 8192;
  u32x4 phq_pos[2], phq_neg[2];
  if (kh == 1) {
    {
      const int rsub = lane >> 2, p16 = lane & 3;
#pragma unroll
      for (int i = 0; i < 7; ++i) {
        const int chunk = wr + 4 * i;
        glds16(P.wqkv + (size_t)(16 * chunk + rsub) * 1344 + c0 + 8 * p16, sm + B_W + chunk * 1024);
      }
    }
    acc_store(cslot, accK, lane); acc_store(cslot + 4096, accV, lane);
    bar_lds();
    __builtin_amdgcn_sched_barrier(0);
    const GAS bf16x8* xq = xp;
    asm volatile("" : "+v"(xq));
    bf16x8 xa[14], xb[14];
#pragma unroll
    for (int t = 0; t < 14; ++t) xa[t] = xq[2 * t];
    wait_vm0();
    { unsigned tg = 0u; bar4(sm + B_CNT + 4, tg); }
    f32x16 accQ;
#pragma unroll
    for (int r = 0; r < 16; ++r) accQ[r] = 0.f;
    const char* qb = sm + B_W + (8 * hh + q4) * 64 + 32 * cb + 8 * p4;
    bf16x8 qf[3];
    qf[0] = tr_frag(qb, qb + 256); qf[1] = tr_frag(qb + 1024, qb + 1024 + 256);
#pragma unroll
    for (int t = 0; t < 28; ++t) {
      if (t + 2 < 28) qf[(t + 2) % 3] = tr_frag(qb + (t + 2) * 1024, qb + (t + 2) * 1024 + 256);
      accQ = MFMA32(qf[t % 3], (t < 14) ? xa[t < 14 ? t : 0] : xb[t >= 14 ? t - 14 : 0], accQ);
      if (t == 1 || t == 6 || t == 11) {
#pragma unroll
        for (int i = t - 1; i < t + 4; ++i) if (i < 14) xb[i] = xq[28 + 2 * i];
      }
      __builtin_amdgcn_sched_barrier(0);
    }
    f32x16 pq;
#pragma unroll
    for (int r = 0; r < 16; ++r) pq[r] = 0.f;
    pq = MFMA32(tr_frag(wpb, wpb + 512), pack_acc(accQ, 0), pq);
    pq = MFMA32(tr_frag(wpb + 1024, wpb + 1536), pack_acc(accQ, 1), pq);
    dual_softmax(pq, hh, phq_pos, phq_neg);
  } else {
    bar_lds();
    acc_add(cslot, accK, lane); acc_add(cslot + 4096, accV, lane);
    f32x16 pk;
#pragma unroll
    for (int r = 0; r < 16; ++r) pk[r] = 0.f;
    pk = MFMA32(tr_frag(wpb, wpb + 512), pack_acc(accK, 0), pk);
    pk = MFMA32(tr_frag(wpb + 1024, wpb + 1536), pack_acc(accK, 1), pk);
    u32x4 kpos[2], kneg[2];
    dual_softmax(pk, hh, kpos, kneg);
    char* phw = sm + B_PHI + wr * B_PHIW;
#pragma unroll
    for (int gq = 0; gq < 4; ++gq) {
      *(u32x2*)(phw + c32 * 136 + (8 * gq + 4 * hh) * 2) = u32x2{kpos[gq >> 1][2 * (gq & 1)], kpos[gq >> 1][2 * (gq & 1) + 1]};
      *(u32x2*)(phw + c32 * 136 + 64 + (8 * gq + 4 * hh) * 2) = u32x2{kneg[gq >> 1][2 * (gq & 1)], kneg[gq >> 1][2 * (gq & 1) + 1]};
    }
    {
      const int cone = off ? 0 : 28;
      const bool valid = (c32 >= off) && (c32 < off + 28);
#pragma unroll
      for (int r = 0; r < 16; ++r) accV[r] = valid ? accV[r] : ((c32 == cone) ? 1.f : 0.f);
    }
    const bf16x8 vp0 = pack_acc(accV, 0), vp1 = pack_acc(accV, 1);
    f32x16 kvp[2];
#pragma unroll
    for (int m = 0; m < 2; ++m) {
#pragma unroll
      for (int r = 0; r < 16; ++r) kvp[m][r] = 0.f;
      const char* pb = phw + (4 * hh + q4) * 136 + (32 * m + 16 * cb + 4 * p4) * 2;
      const bf16x8 a0 = tr_frag(pb, pb + 8 * 136);
      const bf16x8 a1 = tr_frag(pb + 16 * 136, pb + 24 * 136);
      kvp[m] = MFMA32(a0, vp0, kvp[m]);
      kvp[m] = MFMA32(a1, vp1, kvp[m]);
    }
    acc_store(cslot, kvp[0], lane); acc_store(cslot + 4096, kvp[1], lane);
    { unsigned tg = 0u; bar4(sm + B_CNT + 8, tg); }
    {
      const unsigned base = WS_KVP + (unsigned)(h * 16 + rt) * 8192u;
#pragma unroll
      for (int i = 0; i < 2; ++i) {
        const int ck = 2 * wr + i;
        const char* q = sm + B_COMB + (ck * 64 + lane) * 16;
        const f32x4 v = (*(const f32x4*)(q) + *(const f32x4*)(q + 8192)) + (*(const f32x4*)(q + 16384) + *(const f32x4*)(q + 24576));
        st16_wt(wsr, base + (ck * 64 + lane) * 16, __builtin_bit_cast(u32x4, v));
      }
    }
    wait_vm0();
    if (lane == 0) {
      if (atomicAdd((unsigned*)(sm + B_CNT), 1u) == 3u) st_flag(P.ws, WS_FKV + 128 * h + 8 * rt, tag);
    }
#pragma unroll
    for (int s = 0; s < 2; ++s)
#pragma unroll
      for (int j = 0; j < 4; ++j) { phq_pos[s][j] = 0u; phq_neg[s][j] = 0u; }
  }
  STAMP(3);
  bar_lds();
  STAMP(4);
  const int rt2 = 4 * xcd + (jj & 3), ct = jj >> 2;
  for (int i = w; i < 49; i += 8) {
    const int id = i * 64 + lane;
    const int row = id / 7, c16 = id - 7 * row;
    glds16(P.wo + (size_t)row * D_ + 56 * ct + 8 * c16, sm + B_W + i * 1024);
  }
  if (tid < 16) *(unsigned*)(sm + B_W + 50176 + tid * 4) = 0u;
  if (w == 0) wait_flags(P.ws, WS_FKV + 128 * h, 16, tag);
  bar_lds();
  STAMP(5);
  {
    f32x4 s = {0.f, 0.f, 0.f, 0.f};
    u32x4 v[16];
#pragma unroll
    for (int r = 0; r < 16; ++r) v[r] = ld16_wt(wsr, WS_KVP + (unsigned)(h * 16 + r) * 8192u + tid * 16);
#pragma unroll
    for (int r = 0; r < 16; ++r) s += __builtin_bit_cast(f32x4, v[r]);
    const int m = tid >> 8, gq = (tid >> 6) & 3;
    const int f = 32 * m + 8 * gq + 4 * hh;
    unsigned short* kl = (unsigned short*)(sm + B_KVL);
    const unsigned p0 = pack_bf16(s[0], s[1]), p1 = pack_bf16(s[2], s[3]);
    kl[(f + 0) * 32 + c32] = (unsigned short)(p0 & 0xffffu);
    kl[(f + 1) * 32 + c32] = (unsigned short)(p0 >> 16);
    kl[(f + 2) * 32 + c32] = (unsigned short)(p1 & 0xffffu);
    kl[(f + 3) * 32 + c32] = (unsigned short)(p1 >> 16);
  }
  bar_lds();
  STAMP(6);
  if (kh == 1) {
    f32x16 num;
#pragma unroll
    for (int r = 0; r < 16; ++r) num[r] = 0.f;
#pragma unroll
    for (int m = 0; m < 2; ++m)
#pragma unroll
      for (int s = 0; s < 2; ++s) {
        const char* pb = sm + B_KVL + (32 * m + 16 * s + 4 * hh + q4) * 64 + (16 * cb + 4 * p4) * 2;
        const bf16x8 a = tr_frag(pb, pb + 8 * 64);
        num = MFMA32(a, as_frag(m == 0 ? phq_pos[s] : phq_neg[s]), num);
      }
    const float dsrc = off ? num[0] : num[12];
    float den = __shfl(dsrc, c32 + (off ? 0 : 32));
    den = bfr(bfr(den) + 1e-6f);
    const float rden = __builtin_amdgcn_rcpf(den);
    u32x2 ov[4];
#pragma unroll
    for (int gq = 0; gq < 4; ++gq) {
      const int d = 8 * gq + 4 * hh - off;
      const bool ok = (d >= 0) && (d < 28);
      ov[gq][0] = ok ? pack_bf16(bfr(num[4 * gq]) * rden, bfr(num[4 * gq + 1]) * rden) : 0u;
      ov[gq][1] = ok ? pack_bf16(bfr(num[4 * gq + 2]) * rden, bfr(num[4 * gq + 3]) * rden) : 0u;
    }
    const unsigned ob = WS_O + (unsigned)((4 * rt + wr) * 32 + 2 * h) * 1024u + lane * 16;
#pragma unroll
    for (int pr = 0; pr < 2; ++pr) {
      const auto r0 = __builtin_amdgcn_permlane32_swap(ov[2 * pr][0], ov[2 * pr + 1][0], false, false);
      const auto r1 = __builtin_amdgcn_permlane32_swap(ov[2 * pr][1], ov[2 * pr + 1][1], false, false);
      st16_wt(wsr, ob + pr * 1024, u32x4{r0[0], r1[0], r0[1], r1[1]});
    }
  }
  wait_vm0();
  bar_lds();
  if (tid == 0) st_flag(P.ws, WS_FO + 128 * rt + 8 * h, tag);
  STAMP(7);
  if (w == 0) wait_flags(P.ws, WS_FO + 128 * (rt2 >> 1), 16, tag);
  bar_lds();
  STAMP(8);
  {
    const int wr2 = w & 1, wc = (w >> 1) & 1;
    bf16x8 of[16];
    const unsigned ob = WS_O + (unsigned)((2 * rt2 + wr2) * 32 + 16 * kh) * 1024u + lane * 16;
#pragma unroll
    for (int t = 0; t < 16; ++t) of[t] = as_frag(ld16_wt(wsr, ob + 1024 * t));
    f32x16 acc;
#pragma unroll
    for (int r = 0; r < 16; ++r) acc[r] = 0.f;
    const char* wb = sm + B_W + (224 * kh + 8 * hh + q4) * 112 + (28 * wc + 16 * cb + 4 * p4) * 2;
#define WROW(t) ((((28 * ((t) >> 1)) & ~7) + 16 * ((t) & 1)) * 112)
    bf16x8 wf[3];
    wf[0] = tr_frag(wb + WROW(0), wb + WROW(0) + 448); wf[1] = tr_frag(wb + WROW(1), wb + WROW(1) + 448);
#pragma unroll
    for (int t = 0; t < 16; ++t) {
      if (t + 2 < 16) wf[(t + 2) % 3] = tr_frag(wb + WROW(t + 2), wb + WROW(t + 2) + 448);
      acc = MFMA32(wf[t % 3], of[t], acc);
      __builtin_amdgcn_sched_barrier(0);
    }
    STAMP(11);
    char* c2 = sm + B_CMB2 + (w & 3) * 4096;
    if (kh == 1) acc_store(c2, acc, lane);
    bar_lds();
    if (kh == 0) {
      acc_add(c2, acc, lane);
      unsigned short* dp = P.out + (size_t)(64 * rt2 + 32 * wr2 + c32) * OUTW + 448 + 56 * ct + 28 * wc;
#pragma unroll
      for (int gq = 0; gq < 4; ++gq) {
        const int c = 8 * gq + 4 * hh;
        if (c < 28) {
          u32x2 v;
          v[0] = pack_bf16(acc[4 * gq], acc[4 * gq + 1]);
          v[1] = pack_bf16(acc[4 * gq + 2], acc[4 * gq + 3]);
          *(u32x2*)(dp + c) = v;
        }
      }
    }
  }
  if (b == 0 && tid == 0) st_flag(P.ws, WS_EB, tag);
  STAMP(9);
}

__global__ void __launch_bounds__(512, 4) fused_kernel(Params p) {
  extern __shared__ __attribute__((aligned(16))) char sm[];
  const int b = blockIdx.x;
  if (b < 224) part_a(sm, p, b);
  else part_b(sm, p, b - 224);
}

extern "C" void kernel_launch(void* const* d_in, const int* in_sizes, int n_in,
                              void* d_out, int out_size, void* d_ws, size_t ws_size,
                              hipStream_t stream) {
  Params p;
  p.u = (const unsigned short*)d_in[0];
  p.logA = (const unsigned short*)d_in[1];
  p.Aim = (const unsigned short*)d_in[2];
  p.Cre = (const unsigned short*)d_in[3];
  p.Cim = (const unsigned short*)d_in[4];
  p.logdt = (const unsigned short*)d_in[5];
  p.Dsk = (const unsigned short*)d_in[6];
  p.x = (const unsigned short*)d_in[7];
  p.wqkv = (const unsigned short*)d_in[8];
  p.wphi = (const unsigned short*)d_in[9];
  p.wo = (const unsigned short*)d_in[10];
  p.out = (unsigned short*)d_out;
  p.ws = (char*)d_ws;
  (void)hipFuncSetAttribute((const void*)fused_kernel, hipFuncAttributeMaxDynamicSharedMemorySize, LDS_BYTES);
  fused_kernel<<<480, 512, LDS_BYTES, stream>>>(p);
}
```

```cpp
#include <hip/hip_runtime.h>
#include <hip/hip_bf16.h>
#include <stdint.h>

using bf16x8 = __attribute__((ext_vector_type(8))) short;
using s16x4  = __attribute__((ext_vector_type(4))) short;
using f32x16 = __attribute__((ext_vector_type(16))) float;
using f32x4  = __attribute__((ext_vector_type(4))) float;
using u32x4  = __attribute__((ext_vector_type(4))) unsigned;
using u32x2  = __attribute__((ext_vector_type(2))) unsigned;
typedef __bf16 bfx2_t __attribute__((ext_vector_type(2)));
typedef float  f32x2  __attribute__((ext_vector_type(2)));
typedef unsigned long long u64;

#define DI __device__ __forceinline__
#define LAS __attribute__((address_space(3)))
#define GAS __attribute__((address_space(1)))
#define MFMA32(a, b, c) __builtin_amdgcn_mfma_f32_32x32x16_bf16((a), (b), (c), 0, 0, 0)
#define MFMA16(a, b, c) __builtin_amdgcn_mfma_f32_16x16x32_bf16((a), (b), (c), 0, 0, 0)

constexpr int D_  = 448;
constexpr int N_  = 256;
constexpr int OUTW = 896;

#define STAMP(i)
#define WSTAMP(i)

constexpr unsigned WS_EB  = 0;
constexpr unsigned WS_EA  = 256;
constexpr unsigned WS_FKV = 4096;
constexpr unsigned WS_FO  = 8192;
constexpr unsigned WS_FUT = 12288;
constexpr unsigned WS_KVP = 1u << 20;
constexpr unsigned WS_O   = 4u << 20;
constexpr unsigned WS_UT  = 8u << 20;
constexpr unsigned WS_END = 10u << 20;

DI unsigned pack_bf16(float lo, float hi) {
  f32x2 v = {lo, hi};
  return __builtin_bit_cast(unsigned, __builtin_convertvector(v, bfx2_t));
}
DI float bf_lo(unsigned p) { return __uint_as_float(p << 16); }
DI float bf_hi(unsigned p) { return __uint_as_float(p & 0xffff0000u); }
DI float bf2f(unsigned short b) { return __uint_as_float(((unsigned)b) << 16); }
DI float bfr(float x) { return bf_lo(pack_bf16(x, 0.f)); }
DI s16x4 tr_read(const char* p) {
  return __builtin_amdgcn_ds_read_tr16_b64_v4i16((LAS s16x4*)(p));
}
DI bf16x8 tr_frag(const char* p0, const char* p1) {
  s16x4 a = tr_read(p0), b = tr_read(p1);
  return __builtin_shufflevector(a, b, 0, 1, 2, 3, 4, 5, 6, 7);
}
DI bf16x8 as_frag(u32x4 v) { return __builtin_bit_cast(bf16x8, v); }
DI void glds16(const void* g, void* lds_wave_base) {
  __builtin_amdgcn_global_load_lds((const unsigned*)g, (unsigned*)lds_wave_base, 16, 0, 0);
}
DI void wait_vm0() { asm volatile("s_waitcnt vmcnt(0)" ::: "memory"); }
DI void bar_lds() { asm volatile("s_waitcnt lgkmcnt(0)" ::: "memory"); __builtin_amdgcn_s_barrier(); asm volatile("" ::: "memory"); }

typedef __amdgpu_buffer_rsrc_t rsrc_t;
DI void st16_wt(rsrc_t r, unsigned off, u32x4 v) { __builtin_amdgcn_raw_buffer_store_b128(v, r, off, 0, 16); }
DI u32x4 ld16_wt(rsrc_t r, unsigned off) { return __builtin_amdgcn_raw_buffer_load_b128(r, off, 0, 16); }
DI unsigned ld4_wt(rsrc_t r, unsigned off) { return __builtin_amdgcn_raw_buffer_load_b32(r, off, 0, 16); }
DI u64 ld_flag(const char* ws, unsigned off) {
  return __hip_atomic_load((GAS u64*)(ws + off), __ATOMIC_RELAXED, __HIP_MEMORY_SCOPE_AGENT);
}
DI void st_flag(char* ws, unsigned off, u64 v) {
  __hip_atomic_store((GAS u64*)(ws + off), v, __ATOMIC_RELAXED, __HIP_MEMORY_SCOPE_AGENT);
}
DI void wait_flags(const char* ws, unsigned off, int n, u64 tag) {
  const int lane = threadIdx.x & 63;
  for (int it = 0; it < (1 << 16); ++it) {
    const u64 v = (lane < n) ? ld_flag(ws, off + 8 * lane) : tag;
    if (__all(v == tag)) break;
    __builtin_amdgcn_s_sleep(1);
  }
  asm volatile("" ::: "memory");
}
DI u64 uniform64(u64 v) {
  const unsigned lo = __builtin_amdgcn_readfirstlane((unsigned)v), hi = __builtin_amdgcn_readfirstlane((unsigned)(v >> 32));
  return ((u64)hi << 32) | lo;
}

struct Params {
  const unsigned short *u, *logA, *Aim, *Cre, *Cim, *logdt, *Dsk, *x, *wqkv, *wphi, *wo;
  unsigned short* out;
  char* ws;
};

constexpr int LDS_BYTES = 80 * 1024;

constexpr int A_UCOPY = 4160;
constexpr int A_US    = 0;
constexpr int A_CH0   = 4 * A_UCOPY;
constexpr int A_KRH   = 0;
constexpr int A_KRL   = 4576;
constexpr int A_KD    = 9152;
constexpr int A_PR2   = A_KD + 4112;
constexpr int A_RED   = A_PR2 + 2048;
constexpr int A_SUM   = A_RED + 16384;
constexpr int A_CHS   = A_SUM + 64;
constexpr int A_CNT   = A_CH0 + 2 * A_CHS;
static_assert(A_CNT + 16 <= LDS_BYTES, "A lds");

DI void cexp_eval(float lr, float li, float t, float& er, float& ei) {
  const float xr = lr * t;
  const float e  = __builtin_amdgcn_exp2f(xr * 1.44269504088896340736f);
  const float x  = li * t;
  const float C_HI = 0.15915494309189533577f;
  const float C_LO = (float)(0.15915494309189533577 - (double)0.15915494309189533577f);
  const float rh = x * C_HI;
  float rl = __builtin_fmaf(x, C_HI, -rh);
  rl = __builtin_fmaf(x, C_LO, rl);
  const float rev = (rh - __builtin_rintf(rh)) + rl;
  er = e * __builtin_amdgcn_cosf(rev);
  ei = e * __builtin_amdgcn_sinf(rev);
}
DI void cexp_fast(const f32x4& p, float t, float& er, float& ei) {
  const float e = __builtin_amdgcn_exp2f(p[0] * t);
  const float rev = __builtin_fmaf(t, p[2], __builtin_amdgcn_fractf(t * p[1]));
  er = e * __builtin_amdgcn_cosf(rev);
  ei = e * __builtin_amdgcn_sinf(rev);
}
DI void split2v(const f32x2& R, const f32x2& I, u32x4& h, u32x4& l, int q, unsigned mlo, unsigned mhi) {
  const unsigned h0 = pack_bf16(R[0], I[0]), h1 = pack_bf16(R[1], I[1]);
  h[2 * q] = h0; h[2 * q + 1] = h1;
  float r0, i0, r1, i1;
  asm("v_dot2_f32_bf16 %0, %4, %6, %8\n\tv_dot2_f32_bf16 %1, %4, %7, %9\n\t"
      "v_dot2_f32_bf16 %2, %5, %6, %10\n\tv_dot2_f32_bf16 %3, %5, %7, %11\n\ts_nop 2"
      : "=&v"(r0), "=&v"(i0), "=&v"(r1), "=&v"(i1)
      : "v"(h0), "v"(h1), "s"(mlo), "s"(mhi), "v"(R[0]), "v"(I[0]), "v"(R[1]), "v"(I[1]));
  l[2 * q] = pack_bf16(r0, i0);
  l[2 * q + 1] = pack_bf16(r1, i1);
}
DI void bar4(char* cnt, unsigned& target) {
  asm volatile("s_waitcnt lgkmcnt(0)" ::: "memory");
  target += 4u;
  if ((threadIdx.x & 63) == 0) __hip_atomic_fetch_add((LAS unsigned*)cnt, 1u, __ATOMIC_RELAXED, __HIP_MEMORY_SCOPE_WORKGROUP);
  while ((unsigned)__builtin_amdgcn_readfirstlane(__hip_atomic_load((LAS unsigned*)cnt, __ATOMIC_RELAXED, __HIP_MEMORY_SCOPE_WORKGROUP)) < target)
    __builtin_amdgcn_s_sleep(1);
  asm volatile("" ::: "memory");
}

constexpr int bf16_round_int(int t) {
  if (t < 256) return t;
  int sh = 0;
  while ((t >> sh) >= 256) ++sh;
  const int q = t >> sh, r = t & ((1 << sh) - 1), half = 1 << (sh - 1);
  return (q + ((r > half || (r == half && (q & 1))) ? 1 : 0)) << sh;
}
constexpr int kd_slot(int tap) {
  if (tap < 0) return 1025;
  const int p = bf16_round_int(tap);
  if (p < 256) return p;
  if (p >= 2048) return 1024;
  int s = 0;
  while ((p >> s) >= 256) ++s;
  return s * 256 + (p >> s);
}
struct KTab { unsigned short v[2304]; };
constexpr KTab make_ktab() {
  KTab t{};
  for (int i = 0; i < 2304; ++i) t.v[i] = (unsigned short)(4 * kd_slot(2047 - i));
  return t;
}
__device__ __attribute__((aligned(16))) const KTab KTAB = make_ktab();

DI void part_a(char* sm, const Params& P, int a) {
  const int tid = threadIdx.x, lane = tid & 63, w = __builtin_amdgcn_readfirstlane(tid >> 6);
  const int ch = w >> 2, cw = w & 3, ctid = tid & 255;
  const int i16 = lane & 15, g = lane >> 4;
  const int d0 = 56 * (a & 7) + 2 * (a >> 3);
  const int cg = d0 >> 6, r32 = (d0 & 63) >> 1;
  const int d = d0 + ch;
  char* cs = sm + A_CH0 + ch * A_CHS;
  const rsrc_t wsr = __builtin_amdgcn_make_buffer_rsrc(P.ws, 0, WS_END, 0x00020000);
  STAMP(0);
  const u64 ep = ld_flag(P.ws, WS_EA + 256 * cg);
  const unsigned short pla = P.logA[d * N_ + ctid], pai = P.Aim[d * N_ + ctid];
  const unsigned short pcr = P.Cre[d * N_ + ctid], pci = P.Cim[d * N_ + ctid];
  const unsigned short pdt = P.logdt[d];
  const u32x4 ut = *(const u32x4*)(P.u + (size_t)(64 * r32 + (tid >> 3)) * D_ + 64 * cg + 8 * (tid & 7));
  {
    const float dt = bfr(expf(bf2f(pdt)));
    const float am = bfr(expf(bf2f(pla)));
    const float lr = -am * dt, li = bf2f(pai) * dt;
    const float cr = bf2f(pcr) * dt, ci = bf2f(pci) * dt;
    {
      const float C_HI = 0.15915494309189533577f;
      const float C_LO = (float)(0.15915494309189533577 - (double)0.15915494309189533577f);
      const float q = li * C_HI;
      const float er = __builtin_fmaf(li, C_LO, __builtin_fmaf(li, C_HI, -q));
      const float a16 = __uint_as_float(__float_as_uint(q) & 0xffffff00u);
      *(f32x4*)(cs + A_KD + ctid * 16) = f32x4{lr * 1.44269504088896340736f, a16, (q - a16) + er, 0.f};
      *(f32x2*)(cs + A_PR2 + ctid * 8) = f32x2{cr, ci};
    }
    float er, ei;
    cexp_eval(lr, li, 2048.f, er, ei);
    float t = cr * er - ci * ei;
#pragma unroll
    for (int o = 32; o >= 1; o >>= 1) t += __shfl_xor(t, o);
    if (lane == 0) ((float*)(cs + A_SUM))[cw] = t;
  }
  const u64 tag = uniform64(ep) + 1ull;
  {
    unsigned short* T = (unsigned short*)(sm + A_US);
    const int row = tid >> 3, c8 = 8 * (tid & 7);
#pragma unroll
    for (int j = 0; j < 4; ++j) {
      T[(c8 + 2 * j) * 72 + row] = (unsigned short)(ut[j] & 0xffffu);
      T[(c8 + 2 * j + 1) * 72 + row] = (unsigned short)(ut[j] >> 16);
    }
    if (tid < 3) *(unsigned*)(sm + A_CNT + 4 * tid) = 0u;
    bar_lds();
    if (w < 4) {
#pragma unroll
      for (int i = 0; i < 2; ++i) {
        const int c = (tid >> 3) + 32 * i;
        const u32x4 v = *(const u32x4*)(T + c * 72 + 8 * (tid & 7));
        st16_wt(wsr, WS_UT + ((64 * cg + c) * 2048 + 64 * r32 + 8 * (tid & 7)) * 2, v);
      }
    }
  }
  STAMP(1);
  u32x4 uv, kt0, kt1; unsigned unx = 0u;
  char* cb4 = sm + A_CNT + 4 + 4 * ch;
  unsigned b4t = 0u;
  {
    f32x4 acc[4];
#pragma unroll
    for (int s = 0; s < 4; ++s) acc[s] = f32x4{0.f, 0.f, 0.f, 0.f};
    const float ty = (float)i16, tx = (float)(16 * i16);
    unsigned mlo = 0x0000bf80u, mhi = 0xbf800000u;
    asm volatile("" : "+s"(mlo), "+s"(mhi));
    WSTAMP(0);
#pragma unroll 1
    for (int ks = 0; ks < 4; ++ks) {
      if (ks == 3) WSTAMP(1);
      if (ks == 1 && w < 4) {
        wait_vm0();
        if (lane == 0 && atomicAdd((unsigned*)(sm + A_CNT), 1u) == 3u) st_flag(P.ws, WS_FUT + 256 * cg + 8 * r32, tag);
      }
      if (ks == 3) {
        wait_flags(P.ws, WS_FUT + 256 * cg, 32, tag);
        uv = ld16_wt(wsr, WS_UT + (d * 2048 + 8 * ctid) * 2);
        if (ctid < 255) unx = ld4_wt(wsr, WS_UT + (d * 2048 + 8 * ctid + 8) * 2);
        kt0 = *(const u32x4*)(KTAB.v + 8 * ctid);
        kt1 = *(const u32x4*)(KTAB.v + 2048 + 8 * (ctid & 31));
      }
      f32x2 YR[2], YI[2], XR[2], XI[2], CR[2], CI[2];
      const char* pp = cs + A_KD + (64 * cw + 16 * ks + 4 * g) * 16;
      const char* pc = cs + A_PR2 + (64 * cw + 16 * ks + 4 * g) * 8;
#pragma unroll
      for (int m = 0; m < 4; ++m) {
        const f32x4 p = *(const f32x4*)(pp + m * 16);
        float r0, i0, r1, i1;
        cexp_fast(p, ty, r0, i0);
        cexp_fast(p, tx, r1, i1);
        YR[m >> 1][m & 1] = r0; YI[m >> 1][m & 1] = i0;
        XR[m >> 1][m & 1] = r1; XI[m >> 1][m & 1] = i1;
      }
#pragma unroll
      for (int q = 0; q < 2; ++q) {
        const f32x4 c = *(const f32x4*)(pc + q * 16);
        CR[q] = f32x2{c[0], c[2]}; CI[q] = f32x2{c[1], c[3]};
      }
#pragma unroll
      for (int s = 0; s < 4; ++s) {
        u32x4 ah, al, bh, bl;
#pragma unroll
        for (int q = 0; q < 2; ++q) {
          const f32x2 zr = CR[q] * XR[q] - CI[q] * XI[q];
          const f32x2 zn = -(CR[q] * XI[q]) - CI[q] * XR[q];
          split2v(YR[q], YI[q], ah, al, q, mlo, mhi);
          split2v(zr, zn, bh, bl, q, mlo, mhi);
        }
        acc[s] = MFMA16(as_frag(ah), as_frag(bh), acc[s]);
        acc[s] = MFMA16(as_frag(ah), as_frag(bl), acc[s]);
        acc[s] = MFMA16(as_frag(al), as_frag(bh), acc[s]);
        if (s < 3) {
#pragma unroll
          for (int q = 0; q < 2; ++q) {
            const f32x2 a = YR[q], b = YI[q], c = XR[q], e = XI[q];
            YR[q] = a * a - b * b; YI[q] = (a + a) * b;
            XR[q] = c * c - e * e; XI[q] = (c + c) * e;
          }
        }
      }
    }
    WSTAMP(2);
#pragma unroll
    for (int s = 0; s < 4; ++s)
      *(f32x4*)(cs + A_RED + ((cw * 4 + s) * 256 + 16 * i16 + 4 * g) * 4) = acc[s];
  }
  bar4(cb4, b4t);
  STAMP(2);
  {
    const float* red = (const float*)(cs + A_RED);
    unsigned* kp = (unsigned*)(cs + A_KD);
#pragma unroll
    for (int s = 0; s < 4; ++s) {
      const float v = 2.f * ((red[(0 * 4 + s) * 256 + ctid] + red[(1 * 4 + s) * 256 + ctid]) +
                             (red[(2 * 4 + s) * 256 + ctid] + red[(3 * 4 + s) * 256 + ctid]));
      const float vh = bfr(v);
      kp[s * 256 + ctid] = pack_bf16(vh, v - vh);
    }
    if (ctid == 0) {
      const float* sp = (const float*)(cs + A_SUM);
      const float v = 2.f * ((sp[0] + sp[1]) + (sp[2] + sp[3]));
      const float vh = bfr(v);
      kp[1024] = pack_bf16(vh, v - vh);
      kp[1025] = 0u;
    }
  }
  bar4(cb4, b4t);
  {
    const char* kp = cs + A_KD;
#pragma unroll
    for (int part = 0; part < 2; ++part) {
      if (part == 0 || ctid < 30) {
        const u32x4 kt = part ? kt1 : kt0;
        u32x4 h, l;
#pragma unroll
        for (int j = 0; j < 4; ++j) {
          const unsigned a = *(const unsigned*)(kp + (kt[j] & 0xffffu));
          const unsigned b = *(const unsigned*)(kp + (kt[j] >> 16));
          h[j] = __builtin_amdgcn_perm(b, a, 0x05040100u);
          l[j] = __builtin_amdgcn_perm(b, a, 0x07060302u);
        }
        const int c = ctid + 256 * part;
        *(u32x4*)(cs + A_KRH + c * 16) = h;
        *(u32x4*)(cs + A_KRL + c * 16) = l;
      }
    }
  }
  {
    char* c0p = sm + A_US + (2 * ch) * A_UCOPY;
    char* c1p = c0p + A_UCOPY;
    *(u32x4*)(c0p + (8 * ctid + 16) * 2) = uv;
    u32x4 sh;
    sh[0] = (uv[0] >> 16) | (uv[1] << 16); sh[1] = (uv[1] >> 16) | (uv[2] << 16);
    sh[2] = (uv[2] >> 16) | (uv[3] << 16); sh[3] = (uv[3] >> 16) | (unx << 16);
    *(u32x4*)(c1p + (8 * ctid + 16) * 2) = sh;
    if (ctid == 0) {
      *(u32x4*)(c0p) = u32x4{0u, 0u, 0u, 0u}; *(u32x4*)(c0p + 16) = u32x4{0u, 0u, 0u, 0u};
      *(u32x4*)(c1p) = u32x4{0u, 0u, 0u, 0u}; *(u32x4*)(c1p + 16) = u32x4{0u, 0u, 0u, uv[0] << 16};
    }
  }
  bar4(cb4, b4t);
  STAMP(3);
  f32x4 acc[8];
  {
#pragma unroll
    for (int q = 0; q < 8; ++q) acc[q] = f32x4{0.f, 0.f, 0.f, 0.f};
    const int par = (i16 + 1) & 1;
    const char* ubase = sm + A_US + (2 * ch + par) * A_UCOPY + 2 * (i16 + 1 - par + 8 * g);
#pragma unroll 1
    for (int cc = 0; cc < 2; ++cc) {
      const int r = 2 * cw + cc;
      bf16x8 af[8];
#pragma unroll
      for (int q = 0; q < 8; ++q) {
        const unsigned* p = (const unsigned*)(ubase + 64 * (r + 8 * q));
        u32x4 v; v[0] = p[0]; v[1] = p[1]; v[2] = p[2]; v[3] = p[3];
        af[q] = as_frag(v);
      }
      const int kb = 2 * (2032 - 16 * i16 + 32 * r + 8 * g);
#pragma unroll
      for (int b = 0; b < 8; ++b) {
        const bf16x8 bh = *(const bf16x8*)(cs + A_KRH + kb - 512 * b);
        const bf16x8 bl = *(const bf16x8*)(cs + A_KRL + kb - 512 * b);
#pragma unroll
        for (int q = 0; q + b < 8; ++q) acc[q + b] = MFMA16(af[q], bh, acc[q + b]);
#pragma unroll
        for (int q = 0; q + b < 8; ++q) acc[q + b] = MFMA16(af[q], bl, acc[q + b]);
      }
    }
  }
  STAMP(4);
  const unsigned short pds = P.Dsk[d];
  f32x4 y2[2];
  {
    const bool up = (cw & 2) != 0;
    f32x4 keep[4];
#pragma unroll
    for (int i = 0; i < 4; ++i) {
      keep[i] = up ? acc[4 + i] : acc[i];
      const f32x4 snd = up ? acc[i] : acc[4 + i];
      *(f32x4*)(cs + A_RED + ((cw * 4 + i) * 64 + lane) * 16) = snd;
    }
    bar4(cb4, b4t);
#pragma unroll
    for (int i = 0; i < 4; ++i) keep[i] += *(const f32x4*)(cs + A_RED + (((cw ^ 2) * 4 + i) * 64 + lane) * 16);
    const bool od = (cw & 1) != 0;
#pragma unroll
    for (int i = 0; i < 2; ++i) {
      y2[i] = od ? keep[2 + i] : keep[i];
      const f32x4 snd = od ? keep[i] : keep[2 + i];
      *(f32x4*)(cs + A_KRH + ((cw * 2 + i) * 64 + lane) * 16) = snd;
    }
    bar4(cb4, b4t);
#pragma unroll
    for (int i = 0; i < 2; ++i) y2[i] += *(const f32x4*)(cs + A_KRH + (((cw ^ 1) * 2 + i) * 64 + lane) * 16);
  }
  {
    const float ds = bf2f(pds);
    const unsigned short* uc = (const unsigned short*)(sm + A_US + (2 * ch) * A_UCOPY) + 16;
#pragma unroll
    for (int i = 0; i < 2; ++i) {
      const int l = 256 * (4 * (cw >> 1) + 2 * (cw & 1) + i) + 16 * i16 + 4 * g;
      const u32x2 uu = *(const u32x2*)(uc + l);
      u32x2 o;
      o[0] = pack_bf16(bfr(y2[i][0]) + bfr(bf_lo(uu[0]) * ds), bfr(y2[i][1]) + bfr(bf_hi(uu[0]) * ds));
      o[1] = pack_bf16(bfr(y2[i][2]) + bfr(bf_lo(uu[1]) * ds), bfr(y2[i][3]) + bfr(bf_hi(uu[1]) * ds));
      *(u32x2*)(cs + A_KD + l * 2) = o;
    }
  }
  bar_lds();
  {
    const unsigned short* o0 = (const unsigned short*)(sm + A_CH0 + A_KD);
    const unsigned short* o1 = (const unsigned short*)(sm + A_CH0 + A_CHS + A_KD);
#pragma unroll
    for (int i = 0; i < 4; ++i) {
      const int l = tid + 512 * i;
      *(unsigned*)(P.out + (size_t)l * OUTW + d0) = (unsigned)o0[l] | ((unsigned)o1[l] << 16);
    }
  }
  if (r32 == 0 && tid == 0) st_flag(P.ws, WS_EA + 256 * cg, tag);
  STAMP(5);
}

DI bf16x8 pack_acc(const f32x16& x, int s) {
  u32x4 p;
#pragma unroll
  for (int j = 0; j < 4; ++j) p[j] = pack_bf16(x[8 * s + 2 * j], x[8 * s + 2 * j + 1]);
  return as_frag(p);
}

DI void dual_softmax(const f32x16& pacc, int hh, u32x4 (&pos)[2], u32x4 (&neg)[2]) {
  f32x2 p[8];
#pragma unroll
  for (int k = 0; k < 8; ++k) {
    const unsigned b = pack_bf16(pacc[2 * k], pacc[2 * k + 1]);
    p[k] = f32x2{bf_lo(b), bf_hi(b)};
  }
  float mx = fmaxf(p[0][0], p[0][1]), mn = fminf(p[0][0], p[0][1]);
#pragma unroll
  for (int k = 1; k < 6; ++k) { mx = fmaxf(fmaxf(mx, p[k][0]), p[k][1]); mn = fminf(fminf(mn, p[k][0]), p[k][1]); }
  {
    const float m2 = fmaxf(fmaxf(p[6][0], p[6][1]), fmaxf(p[7][0], p[7][1]));
    const float n2 = fminf(fminf(p[6][0], p[6][1]), fminf(p[7][0], p[7][1]));
    mx = hh ? mx : fmaxf(mx, m2); mn = hh ? mn : fminf(mn, n2);
  }
  mx = fmaxf(mx, __shfl_xor(mx, 32));
  mn = fminf(mn, __shfl_xor(mn, 32));
  const float L2E = 1.44269504088896340736f;
  const f32x2 mxl = {mx * L2E, mx * L2E}, mnl = {mn * L2E, mn * L2E};
  f32x2 ep[8], en[8], sp2 = {0.f, 0.f}, sn2 = {0.f, 0.f};
#pragma unroll
  for (int k = 0; k < 8; ++k) {
    const f32x2 a = p[k] * L2E;
    const f32x2 x = a - mxl, y = mnl - a;
    ep[k] = f32x2{__builtin_amdgcn_exp2f(x[0]), __builtin_amdgcn_exp2f(x[1])};
    en[k] = f32x2{__builtin_amdgcn_exp2f(y[0]), __builtin_amdgcn_exp2f(y[1])};
    if (k >= 6) { ep[k] = hh ? f32x2{0.f, 0.f} : ep[k]; en[k] = hh ? f32x2{0.f, 0.f} : en[k]; }
    sp2 += ep[k]; sn2 += en[k];
  }
  float sp = sp2[0] + sp2[1], sn = sn2[0] + sn2[1];
  sp += __shfl_xor(sp, 32);
  sn += __shfl_xor(sn, 32);
  const float rp = __builtin_amdgcn_rcpf(sp), rn = __builtin_amdgcn_rcpf(sn);
#pragma unroll
  for (int k = 0; k < 8; ++k) {
    const f32x2 a = ep[k] * rp, b = en[k] * rn;
    pos[k >> 2][k & 3] = pack_bf16(a[0], a[1]);
    neg[k >> 2][k & 3] = pack_bf16(b[0], b[1]);
  }
}
DI void acc_store(char* p, const f32x16& a, int lane) {
#pragma unroll
  for (int gq = 0; gq < 4; ++gq)
    *(f32x4*)(p + (gq * 64 + lane) * 16) = f32x4{a[4 * gq], a[4 * gq + 1], a[4 * gq + 2], a[4 * gq + 3]};
}
DI void acc_add(const char* p, f32x16& a, int lane) {
#pragma unroll
  for (int gq = 0; gq < 4; ++gq) {
    const f32x4 v = *(const f32x4*)(p + (gq * 64 + lane) * 16);
    a[4 * gq] += v[0]; a[4 * gq + 1] += v[1]; a[4 * gq + 2] += v[2]; a[4 * gq + 3] += v[3];
  }
}

constexpr int B_W    = 0;
constexpr int B_COMB = 28672;
constexpr int B_PHI  = 61440;
constexpr int B_PHIW = 32 * 136;
constexpr int B_KVL  = 61440;
constexpr int B_WPH  = 79872;
constexpr int B_XS   = 57344;
constexpr int B_CNT  = 79360;
constexpr int B_CMB2 = 53248;

DI void part_b(char* sm, const Params& P, int b) {
  const int tid = threadIdx.x, lane = tid & 63, w = __builtin_amdgcn_readfirstlane(tid >> 6);
  const int wr = w & 3, kh = w >> 2;
  const int c32 = lane & 31, hh = lane >> 5;
  const int xcd = b & 7, jj = b >> 3;
  const int rt = 4 * (xcd & 3) + (jj & 3), h = 8 * (xcd >> 2) + (jj >> 2);
  const int off = (28 * h) & 7, c0 = 28 * h - off;
  const int q4 = (lane & 15) >> 2, p4 = lane & 3, cb = (lane >> 4) & 1;
  const rsrc_t wsr = __builtin_amdgcn_make_buffer_rsrc(P.ws, 0, WS_END, 0x00020000);
  STAMP(0);
  const u64 ep = ld_flag(P.ws, WS_EB);
  {
    const int rsub = lane >> 3, p16 = lane & 7;
#pragma unroll
    for (int i = 0; i < 7; ++i) {
      int chunk = w + 8 * i + 14 * (jj & 3);
      chunk = chunk >= 56 ? chunk - 56 : chunk;
      const int row = 8 * chunk + rsub;
      const int seg = (p16 >> 2) ^ ((row >> 1) & 1);
      glds16(P.wqkv + (size_t)row * 1344 + 448 + 448 * seg + c0 + 8 * (p16 & 3), sm + B_W + chunk * 1024);
    }
  }
  bf16x8 xf[16];
  int lrot[4];
#pragma unroll
  for (int L = 0; L < 4; ++L) lrot[L] = kh ? (L + (jj >> 2)) % 3 : ((L + (jj >> 2)) & 3);
  const GAS bf16x8* xp = (const GAS bf16x8*)(P.x + (size_t)(128 * rt + 32 * wr + c32) * D_ + 8 * hh);
  {
    u32x4 raw[16];
    const GAS u32x4* xr = (const GAS u32x4*)(P.x + (size_t)(128 * rt + 32 * wr + (lane >> 3)) * D_ + 256 * kh + 8 * (lane & 7));
#pragma unroll
    for (int i = 0; i < 16; ++i)
      if (i < 12 || kh == 0) raw[i] = xr[(i & 3) * 8 * 56 + lrot[i >> 2] * 8];
    char* xs = sm + B_XS + w * 2048;
    const int cq = lane & 3, hsel = (lane >> 2) & 1, r8 = lane >> 3;
    char* wa0 = xs + r8 * 64 + ((cq ^ (r8 >> 2)) * 16);
    char* wa1 = xs + 512 + r8 * 64 + ((cq ^ (2 + (r8 >> 2))) * 16);
    const char* ra0 = xs + c32 * 64 + ((hh ^ ((c32 >> 2) & 3)) * 16);
    const char* ra1 = xs + c32 * 64 + (((2 + hh) ^ ((c32 >> 2) & 3)) * 16);
#pragma unroll
    for (int L = 0; L < 4; ++L) {
      if (L < 3 || kh == 0) {
#pragma unroll
        for (int hf = 0; hf < 2; ++hf) {
          if (hsel == hf) {
            *(u32x4*)(wa0) = raw[4 * L]; *(u32x4*)(wa1) = raw[4 * L + 1];
            *(u32x4*)(wa0 + 1024) = raw[4 * L + 2]; *(u32x4*)(wa1 + 1024) = raw[4 * L + 3];
          }
          __builtin_amdgcn_fence(__ATOMIC_RELEASE, "wavefront"); __builtin_amdgcn_wave_barrier(); asm volatile("" ::: "memory");
          xf[4 * L + 2 * hf] = *(const bf16x8*)(ra0);
          xf[4 * L + 2 * hf + 1] = *(const bf16x8*)(ra1);
          asm volatile("" ::: "memory"); __builtin_amdgcn_wave_barrier();
        }
      }
    }
  }
  {
    const int c = (tid >> 4) - off, f0 = 2 * (tid & 15);
    const bool okc = (c >= 0) && (c < 28) && (f0 < 28);
    const unsigned short* wp = P.wphi + h * 784 + (okc ? c * 28 + f0 : 0);
    const unsigned v = (unsigned)wp[0] | ((unsigned)wp[1] << 16);
    *(unsigned*)(sm + B_WPH + tid * 4) = okc ? v : 0u;
    if (tid < 3) *(unsigned*)(sm + B_CNT + 4 * tid) = 0u;
  }
  wait_vm0();
  const u64 tag = uniform64(ep) + 1ull;
  bar_lds();
  __builtin_amdgcn_s_setprio(2);
  STAMP(1);
  f32x16 accK, accV;
#pragma unroll
  for (int r = 0; r < 16; ++r) { accK[r] = 0.f; accV[r] = 0.f; }
  {
    const int sw = (q4 >> 1) & 1;
    const char* kb = sm + B_W + (256 * kh + 8 * hh + q4) * 128 + (0 ^ sw) * 64 + 32 * cb + 8 * p4;
    const int vo = (sw ? -64 : 64);
    bf16x8 kf[2], vf[2];
    { const char* a = kb + lrot[0] * 8192; kf[0] = tr_frag(a, a + 512); vf[0] = tr_frag(a + vo, a + vo + 512); }
#pragma unroll
    for (int t = 0; t < 16; ++t) {
      if (t < 12 || kh == 0) {
        if (t + 1 < 12 || (t + 1 < 16 && kh == 0)) {
          const char* a = kb + lrot[(t + 1) >> 2] * 8192 + ((t + 1) & 3) * 2048;
          kf[(t + 1) & 1] = tr_frag(a, a + 512);
          vf[(t + 1) & 1] = tr_frag(a + vo, a + vo + 512);
        }
        accK = MFMA32(kf[t & 1], xf[t], accK);
        accV = MFMA32(xf[t], vf[t & 1], accV);
      }
      __builtin_amdgcn_sched_barrier(0);
    }
  }
  bar_lds();
  STAMP(2);
  const char* wpb = sm + B_WPH + (4 * hh + q4) * 64 + (16 * cb + 4 * p4) * 2;
  char* cslot = sm + B_COMB + wr * 8192;
  u32x4 phq_pos[2], phq_neg[2];
  if (kh == 1) {
    {
      const int rsub = lane >> 2, p16 = lane & 3;
#pragma unroll
      for (int i = 0; i < 7; ++i) {
        const int chunk = wr + 4 * i;
        glds16(P.wqkv + (size_t)(16 * chunk + rsub) * 1344 + c0 + 8 * p16, sm + B_W + chunk * 1024);
      }
    }
    acc_store(cslot, accK, lane); acc_store(cslot + 4096, accV, lane);
    bar_lds();
    __builtin_amdgcn_sched_barrier(0);
    const GAS bf16x8* xq = xp;
    asm volatile("" : "+v"(xq));
    bf16x8 xa[14], xb[14];
#pragma unroll
    for (int t = 0; t < 14; ++t) xa[t] = xq[2 * t];
    wait_vm0();
    { unsigned tg = 0u; bar4(sm + B_CNT + 4, tg); }
    f32x16 accQ;
#pragma unroll
    for (int r = 0; r < 16; ++r) accQ[r] = 0.f;
    const char* qb = sm + B_W + (8 * hh + q4) * 64 + 32 * cb + 8 * p4;
    bf16x8 qf[3];
    qf[0] = tr_frag(qb, qb + 256); qf[1] = tr_frag(qb + 1024, qb + 1024 + 256);
#pragma unroll
    for (int t = 0; t < 28; ++t) {
      if (t + 2 < 28) qf[(t + 2) % 3] = tr_frag(qb + (t + 2) * 1024, qb + (t + 2) * 1024 + 256);
      accQ = MFMA32(qf[t % 3], (t < 14) ? xa[t < 14 ? t : 0] : xb[t >= 14 ? t - 14 : 0], accQ);
      if (t == 1 || t == 6 || t == 11) {
#pragma unroll
        for (int i = t - 1; i < t + 4; ++i) if (i < 14) xb[i] = xq[28 + 2 * i];
      }
      __builtin_amdgcn_sched_barrier(0);
    }
    f32x16 pq;
#pragma unroll
    for (int r = 0; r < 16; ++r) pq[r] = 0.f;
    pq = MFMA32(tr_frag(wpb, wpb + 512), pack_acc(accQ, 0), pq);
    pq = MFMA32(tr_frag(wpb + 1024, wpb + 1536), pack_acc(accQ, 1), pq);
    dual_softmax(pq, hh, phq_pos, phq_neg);
  } else {
    bar_lds();
    acc_add(cslot, accK, lane); acc_add(cslot + 4096, accV, lane);
    f32x16 pk;
#pragma unroll
    for (int r = 0; r < 16; ++r) pk[r] = 0.f;
    pk = MFMA32(tr_frag(wpb, wpb + 512), pack_acc(accK, 0), pk);
    pk = MFMA32(tr_frag(wpb + 1024, wpb + 1536), pack_acc(accK, 1), pk);
    u32x4 kpos[2], kneg[2];
    dual_softmax(pk, hh, kpos, kneg);
    char* phw = sm + B_PHI + wr * B_PHIW;
#pragma unroll
    for (int gq = 0; gq < 4; ++gq) {
      *(u32x2*)(phw + c32 * 136 + (8 * gq + 4 * hh) * 2) = u32x2{kpos[gq >> 1][2 * (gq & 1)], kpos[gq >> 1][2 * (gq & 1) + 1]};
      *(u32x2*)(phw + c32 * 136 + 64 + (8 * gq + 4 * hh) * 2) = u32x2{kneg[gq >> 1][2 * (gq & 1)], kneg[gq >> 1][2 * (gq & 1) + 1]};
    }
    {
      const int cone = off ? 0 : 28;
      const bool valid = (c32 >= off) && (c32 < off + 28);
#pragma unroll
      for (int r = 0; r < 16; ++r) accV[r] = valid ? accV[r] : ((c32 == cone) ? 1.f : 0.f);
    }
    const bf16x8 vp0 = pack_acc(accV, 0), vp1 = pack_acc(accV, 1);
    f32x16 kvp[2];
#pragma unroll
    for (int m = 0; m < 2; ++m) {
#pragma unroll
      for (int r = 0; r < 16; ++r) kvp[m][r] = 0.f;
      const char* pb = phw + (4 * hh + q4) * 136 + (32 * m + 16 * cb + 4 * p4) * 2;
      const bf16x8 a0 = tr_frag(pb, pb + 8 * 136);
      const bf16x8 a1 = tr_frag(pb + 16 * 136, pb + 24 * 136);
      kvp[m] = MFMA32(a0, vp0, kvp[m]);
      kvp[m] = MFMA32(a1, vp1, kvp[m]);
    }
    acc_store(cslot, kvp[0], lane); acc_store(cslot + 4096, kvp[1], lane);
    { unsigned tg = 0u; bar4(sm + B_CNT + 8, tg); }
    {
      const unsigned base = WS_KVP + (unsigned)(h * 16 + rt) * 8192u;
#pragma unroll
      for (int i = 0; i < 2; ++i) {
        const int ck = 2 * wr + i;
        const char* q = sm + B_COMB + (ck * 64 + lane) * 16;
        const f32x4 v = (*(const f32x4*)(q) + *(const f32x4*)(q + 8192)) + (*(const f32x4*)(q + 16384) + *(const f32x4*)(q + 24576));
        st16_wt(wsr, base + (ck * 64 + lane) * 16, __builtin_bit_cast(u32x4, v));
      }
    }
    wait_vm0();
    if (lane == 0) {
      if (atomicAdd((unsigned*)(sm + B_CNT), 1u) == 3u) st_flag(P.ws, WS_FKV + 128 * h + 8 * rt, tag);
    }
#pragma unroll
    for (int s = 0; s < 2; ++s)
#pragma unroll
      for (int j = 0; j < 4; ++j) { phq_pos[s][j] = 0u; phq_neg[s][j] = 0u; }
  }
  STAMP(3);
  bar_lds();
  STAMP(4);
  const int rt2 = 4 * xcd + (jj & 3), ct = jj >> 2;
  for (int i = w; i < 49; i += 8) {
    const int id = i * 64 + lane;
    const int row = id / 7, c16 = id - 7 * row;
    glds16(P.wo + (size_t)row * D_ + 56 * ct + 8 * c16, sm + B_W + i * 1024);
  }
  if (tid < 16) *(unsigned*)(sm + B_W + 50176 + tid * 4) = 0u;
  if (w == 0) wait_flags(P.ws, WS_FKV + 128 * h, 16, tag);
  bar_lds();
  STAMP(5);
  {
    f32x4 s = {0.f, 0.f, 0.f, 0.f};
    u32x4 v[16];
#pragma unroll
    for (int r = 0; r < 16; ++r) v[r] = ld16_wt(wsr, WS_KVP + (unsigned)(h * 16 + r) * 8192u + tid * 16);
#pragma unroll
    for (int r = 0; r < 16; ++r) s += __builtin_bit_cast(f32x4, v[r]);
    const int m = tid >> 8, gq = (tid >> 6) & 3;
    const int f = 32 * m + 8 * gq + 4 * hh;
    unsigned short* kl = (unsigned short*)(sm + B_KVL);
    const unsigned p0 = pack_bf16(s[0], s[1]), p1 = pack_bf16(s[2], s[3]);
    kl[(f + 0) * 32 + c32] = (unsigned short)(p0 & 0xffffu);
    kl[(f + 1) * 32 + c32] = (unsigned short)(p0 >> 16);
    kl[(f + 2) * 32 + c32] = (unsigned short)(p1 & 0xffffu);
    kl[(f + 3) * 32 + c32] = (unsigned short)(p1 >> 16);
  }
  bar_lds();
  STAMP(6);
  if (kh == 1) {
    f32x16 num;
#pragma unroll
    for (int r = 0; r < 16; ++r) num[r] = 0.f;
#pragma unroll
    for (int m = 0; m < 2; ++m)
#pragma unroll
      for (int s = 0; s < 2; ++s) {
        const char* pb = sm + B_KVL + (32 * m + 16 * s + 4 * hh + q4) * 64 + (16 * cb + 4 * p4) * 2;
        const bf16x8 a = tr_frag(pb, pb + 8 * 64);
        num = MFMA32(a, as_frag(m == 0 ? phq_pos[s] : phq_neg[s]), num);
      }
    const float dsrc = off ? num[0] : num[12];
    float den = __shfl(dsrc, c32 + (off ? 0 : 32));
    den = bfr(bfr(den) + 1e-6f);
    const float rden = __builtin_amdgcn_rcpf(den);
    u32x2 ov[4];
#pragma unroll
    for (int gq = 0; gq < 4; ++gq) {
      const int d = 8 * gq + 4 * hh - off;
      const bool ok = (d >= 0) && (d < 28);
      ov[gq][0] = ok ? pack_bf16(bfr(num[4 * gq]) * rden, bfr(num[4 * gq + 1]) * rden) : 0u;
      ov[gq][1] = ok ? pack_bf16(bfr(num[4 * gq + 2]) * rden, bfr(num[4 * gq + 3]) * rden) : 0u;
    }
    const unsigned ob = WS_O + (unsigned)((4 * rt + wr) * 32 + 2 * h) * 1024u + lane * 16;
#pragma unroll
    for (int pr = 0; pr < 2; ++pr) {
      const auto r0 = __builtin_amdgcn_permlane32_swap(ov[2 * pr][0], ov[2 * pr + 1][0], false, false);
      const auto r1 = __builtin_amdgcn_permlane32_swap(ov[2 * pr][1], ov[2 * pr + 1][1], false, false);
      st16_wt(wsr, ob + pr * 1024, u32x4{r0[0], r1[0], r0[1], r1[1]});
    }
  }
  wait_vm0();
  bar_lds();
  if (tid == 0) st_flag(P.ws, WS_FO + 128 * rt + 8 * h, tag);
  STAMP(7);
  if (w == 0) wait_flags(P.ws, WS_FO + 128 * (rt2 >> 1), 16, tag);
  bar_lds();
  STAMP(8);
  {
    const int wr2 = w & 1, wc = (w >> 1) & 1;
    bf16x8 of[16];
    const unsigned ob = WS_O + (unsigned)((2 * rt2 + wr2) * 32 + 16 * kh) * 1024u + lane * 16;
#pragma unroll
    for (int t = 0; t < 16; ++t) of[t] = as_frag(ld16_wt(wsr, ob + 1024 * t));
    f32x16 acc;
#pragma unroll
    for (int r = 0; r < 16; ++r) acc[r] = 0.f;
    const char* wb = sm + B_W + (224 * kh + 8 * hh + q4) * 112 + (28 * wc + 16 * cb + 4 * p4) * 2;
#define WROW(t) ((((28 * ((t) >> 1)) & ~7) + 16 * ((t) & 1)) * 112)
    bf16x8 wf[3];
    wf[0] = tr_frag(wb + WROW(0), wb + WROW(0) + 448); wf[1] = tr_frag(wb + WROW(1), wb + WROW(1) + 448);
#pragma unroll
    for (int t = 0; t < 16; ++t) {
      if (t + 2 < 16) wf[(t + 2) % 3] = tr_frag(wb + WROW(t + 2), wb + WROW(t + 2) + 448);
      acc = MFMA32(wf[t % 3], of[t], acc);
      __builtin_amdgcn_sched_barrier(0);
    }
    STAMP(11);
    char* c2 = sm + B_CMB2 + (w & 3) * 4096;
    if (kh == 1) acc_store(c2, acc, lane);
    bar_lds();
    if (kh == 0) {
      acc_add(c2, acc, lane);
      unsigned short* dp = P.out + (size_t)(64 * rt2 + 32 * wr2 + c32) * OUTW + 448 + 56 * ct + 28 * wc;
#pragma unroll
      for (int gq = 0; gq < 4; ++gq) {
        const int c = 8 * gq + 4 * hh;
        if (c < 28) {
          u32x2 v;
          v[0] = pack_bf16(acc[4 * gq], acc[4 * gq + 1]);
          v[1] = pack_bf16(acc[4 * gq + 2], acc[4 * gq + 3]);
          *(u32x2*)(dp + c) = v;
        }
      }
    }
  }
  if (b == 0 && tid == 0) st_flag(P.ws, WS_EB, tag);
  STAMP(9);
}

__global__ void __launch_bounds__(512, 4) fused_kernel(Params p) {
  extern __shared__ __attribute__((aligned(16))) char sm[];
  const int b = blockIdx.x;
  if (b < 224) part_a(sm, p, b);
  else part_b(sm, p, b - 224);
}

extern "C" void kernel_launch(void* const* d_in, const int* in_sizes, int n_in,
                              void* d_out, int out_size, void* d_ws, size_t ws_size,
                              hipStream_t stream) {
  Params p;
  p.u = (const unsigned short*)d_in[0];
  p.logA = (const unsigned short*)d_in[1];
  p.Aim = (const unsigned short*)d_in[2];
  p.Cre = (const unsigned short*)d_in[3];
  p.Cim = (const unsigned short*)d_in[4];
  p.logdt = (const unsigned short*)d_in[5];
  p.Dsk = (const unsigned short*)d_in[6];
  p.x = (const unsigned short*)d_in[7];
  p.wqkv = (const unsigned short*)d_in[8];
  p.wphi = (const unsigned short*)d_in[9];
  p.wo = (const unsigned short*)d_in[10];
  p.out = (unsigned short*)d_out;
  p.ws = (char*)d_ws;
  (void)hipFuncSetAttribute((const void*)fused_kernel, hipFuncAttributeMaxDynamicSharedMemorySize, LDS_BYTES);
  fused_kernel<<<480, 512, LDS_BYTES, stream>>>(p);
}
```
